```python
import torch

def _softmax(x, axis=-1):
    return torch.softmax(x, dim=axis)

def _gelu(x):
    return torch.nn.functional.gelu(x, approximate='tanh')

def _silu(x):
    return torch.nn.functional.silu(x)

def _sigmoid(x):
    return torch.sigmoid(x)

def _layernorm(x, axis=-1, eps=1e-05):
    m = x.mean(dim=axis, keepdim=True)
    v = x.var(dim=axis, keepdim=True, unbiased=False)
    return (x - m) / torch.sqrt(v + eps)

def _scatter_add(tgt, idx, src, axis=0):
    return tgt.clone().index_add_(axis, idx, src)

def _segment_sum(src, seg, n):
    out = torch.zeros((n,) + src.shape[1:], dtype=src.dtype)
    return out.index_add_(0, seg, src)

def _pad(x, pw):
    flat = tuple((v for pair in reversed(pw) for v in pair))
    return torch.nn.functional.pad(x, flat)
kernel_name = 'standalone__tree_speculative_verify_tree_attention_over_candidate_token_tree__sampling'

def setup_inputs():

    _w = lambda a: (lambda t: t.to(torch.bfloat16) if t.is_floating_point() else t)(torch.as_tensor(a))
    B, Hq, Hkv, T, C, D = (32, 32, 8, 8, 2048, 128)
    tree_mask = torch.zeros((B, T, T), dtype=float)
    for b in range(B):
        parent = torch.zeros(T, dtype=torch.int64)
        for i in range(1, T):
            parent[i] = int(torch.randint(0, i, (1,)).item())
        for i in range(T):
            j = i
            tree_mask[b, i, j] = 1.0
            while j > 0:
                j = parent[j]
                tree_mask[b, i, j] = 1.0
    return {'q': _w((lambda *s: torch.randn(*s, dtype=torch.bfloat16) * 0.8)(B, Hq, T, D)), 'k_cache': _w((lambda *s: torch.randn(*s, dtype=torch.bfloat16) * 0.8)(B, Hkv, C, D)), 'v_cache': _w((lambda *s: torch.randn(*s, dtype=torch.bfloat16) * 0.05)(B, Hkv, C, D)), 'k_tree': _w((lambda *s: torch.randn(*s, dtype=torch.bfloat16) * 0.8)(B, Hkv, T, D)), 'v_tree': _w((lambda *s: torch.randn(*s, dtype=torch.bfloat16) * 0.05)(B, Hkv, T, D)), 'tree_mask': _w(tree_mask)}

def reference(q, k_cache, v_cache, k_tree, v_tree, tree_mask):
    B, Hq, T, D = q.shape
    Hkv, C = (k_cache.shape[1], k_cache.shape[2])
    rep = Hq // Hkv
    kc = (lambda x, n, axis: torch.repeat_interleave(x, n, dim=axis))(k_cache, rep, axis=1)
    vc = (lambda x, n, axis: torch.repeat_interleave(x, n, dim=axis))(v_cache, rep, axis=1)
    kt = (lambda x, n, axis: torch.repeat_interleave(x, n, dim=axis))(k_tree, rep, axis=1)
    vt = (lambda x, n, axis: torch.repeat_interleave(x, n, dim=axis))(v_tree, rep, axis=1)
    k = torch.concatenate([kc, kt], axis=2)
    v = torch.concatenate([vc, vt], axis=2)
    scale = D ** (-0.5)
    scores = torch.einsum('bhqd,bhkd->bhqk', q, k) * scale
    ctx_mask = torch.ones((B, 1, T, C), dtype=scores.dtype)
    tm = tree_mask[:, None, :, :].clone()
    full_mask = torch.concatenate([ctx_mask, tm], axis=-1)
    scores = scores + (1.0 - full_mask) * -1000000000.0
    p = _softmax(scores, axis=-1)
    return torch.einsum('bhqk,bhkd->bhqd', p, v)
```

```cpp
#include <hip/hip_runtime.h>
#include <hip/hip_bf16.h>
#include <stdint.h>

#define KV_AUX 2
#define INTERLEAVE 1

namespace {
constexpr int B_ = 32, HQ = 32, HKV = 8, T_ = 8, C_ = 2048, D_ = 128;
constexpr int G_ = HQ / HKV;
constexpr int M_ = G_ * T_;
constexpr int NW = 8;
constexpr int TK = 32;
constexpr int NT = C_ / (NW * TK);
constexpr int TILE_BYTES = TK * D_ * 2;
static_assert(M_ == 32 && D_ == 128, "layout assumes 32 rows x 128 dims");

typedef __attribute__((ext_vector_type(8))) __bf16 bf16x8;
typedef __attribute__((ext_vector_type(4))) float f32x4;
typedef __attribute__((ext_vector_type(4))) unsigned u32x4;
typedef unsigned short u16;

constexpr float LOG2E = 1.4426950408889634f;
constexpr float SCALE = 0.08838834764831845f;
constexpr float CS = SCALE * LOG2E;

__device__ __forceinline__ f32x4 mfma16(u32x4 a, u32x4 b, f32x4 c) {
  return __builtin_amdgcn_mfma_f32_16x16x32_bf16(__builtin_bit_cast(bf16x8, a), __builtin_bit_cast(bf16x8, b), c, 0, 0, 0);
}
__device__ __forceinline__ unsigned pack_bf16(float lo, float hi) {
  typedef __attribute__((ext_vector_type(2))) float f32x2;
  typedef __attribute__((ext_vector_type(2))) __bf16 bf16x2;
  f32x2 v = {lo, hi};
  bf16x2 r = __builtin_convertvector(v, bf16x2);
  return __builtin_bit_cast(unsigned, r);
}
__device__ __forceinline__ float xrow_max(float x) {
  auto s = __builtin_amdgcn_permlane16_swap(__float_as_uint(x), __float_as_uint(x), false, false);
  x = fmaxf(__uint_as_float(s[0]), __uint_as_float(s[1]));
  auto t = __builtin_amdgcn_permlane32_swap(__float_as_uint(x), __float_as_uint(x), false, false);
  return fmaxf(__uint_as_float(t[0]), __uint_as_float(t[1]));
}
__device__ __forceinline__ float xrow_sum(float x) {
  auto s = __builtin_amdgcn_permlane16_swap(__float_as_uint(x), __float_as_uint(x), false, false);
  x = __uint_as_float(s[0]) + __uint_as_float(s[1]);
  auto t = __builtin_amdgcn_permlane32_swap(__float_as_uint(x), __float_as_uint(x), false, false);
  return __uint_as_float(t[0]) + __uint_as_float(t[1]);
}
__device__ __forceinline__ float bf2f(u16 x) { return __uint_as_float((unsigned)x << 16); }

constexpr int E_STRIDE = 64 * 16 + 32;
constexpr int W_STRIDE = 16 * E_STRIDE;
constexpr int ML_OFF = NW * W_STRIDE;
constexpr int LDS_BYTES = ML_OFF + NW * M_ * 8;

__global__ __launch_bounds__(NW * 64, 2) void tree_attn_kernel(const u16* __restrict__ q, const u16* __restrict__ kc,
                                                               const u16* __restrict__ vc, const u16* __restrict__ kt,
                                                               const u16* __restrict__ vt, const u16* __restrict__ mask,
                                                               u16* __restrict__ out) {
  __shared__ __attribute__((aligned(16))) char lds[LDS_BYTES];
  const int bh = blockIdx.x;
  const int tid = threadIdx.x, lane = tid & 63;
  const int wave = __builtin_amdgcn_readfirstlane(tid >> 6);
  const int g = lane >> 4, c = lane & 15;

  const size_t base = (size_t)bh * C_ * D_;
  const int nrec = C_ * D_ * 2;
  const int toff0 = wave * TILE_BYTES, tstep = NW * TILE_BYTES;
  const __amdgpu_buffer_rsrc_t krs = __builtin_amdgcn_make_buffer_rsrc((void*)(kc + base), 0, nrec, 0x00020000);
  const __amdgpu_buffer_rsrc_t vrs = __builtin_amdgcn_make_buffer_rsrc((void*)(vc + base), 0, nrec, 0x00020000);
  const int kap = 4 * (c & 3) + (c >> 2);
  const int kvo = kap * 256 + g * 16;
  const int vvo = lane * 16;

  u32x4 K[2][4], V[8];
#define LOADK(off)                                                                                   \
  do {                                                                                               \
    _Pragma("unroll") for (int u = 0; u < 2; ++u) _Pragma("unroll") for (int s = 0; s < 4; ++s)     \
        K[u][s] = __builtin_amdgcn_raw_buffer_load_b128(krs, kvo + s * 64, (off) + u * 4096, KV_AUX); \
  } while (0)
#define LOADV(off)                                                                                           \
  do {                                                                                                       \
    _Pragma("unroll") for (int j = 0; j < 8; ++j)                                                           \
        V[j] = __builtin_amdgcn_raw_buffer_load_b128(vrs, vvo + (j & 3) * 1024, (off) + (j >> 2) * 4096, KV_AUX); \
  } while (0)

  LOADK(toff0);
  u32x4 Q[2][4];
  {
    const u16* qb = q + (size_t)bh * M_ * D_;
#pragma unroll
    for (int v = 0; v < 2; ++v)
#pragma unroll
      for (int s = 0; s < 4; ++s) Q[v][s] = *reinterpret_cast<const u32x4*>(qb + (16 * v + c) * D_ + 32 * s + 8 * g);
  }
  LOADV(toff0);

  float m[2] = {-INFINITY, -INFINITY}, l[2] = {0.f, 0.f};
  f32x4 O[2][8];
#pragma unroll
  for (int v = 0; v < 2; ++v)
#pragma unroll
    for (int e = 0; e < 8; ++e) O[v][e] = f32x4{0.f, 0.f, 0.f, 0.f};

  if (wave == NW - 1) {
    const u16* ktb = kt + (size_t)bh * T_ * D_;
    const u16* vtb = vt + (size_t)bh * T_ * D_;
    u32x4 Kt[4], Vt[2];
#pragma unroll
    for (int s = 0; s < 4; ++s) Kt[s] = *reinterpret_cast<const u32x4*>(ktb + (kap & 7) * D_ + 32 * s + 8 * g);
#pragma unroll
    for (int j = 0; j < 2; ++j) Vt[j] = *reinterpret_cast<const u32x4*>(vtb + j * 512 + lane * 8);
    const u16* mb = mask + (size_t)(bh / HKV) * T_ * T_ + (c & 7) * T_;
    const float bias0 = (1.0f - bf2f(mb[g])) * -1000000000.0f * LOG2E;
    const float bias1 = (1.0f - bf2f(mb[4 + g])) * -1000000000.0f * LOG2E;
    u32x4 P[2];
#pragma unroll
    for (int v = 0; v < 2; ++v) {
      f32x4 S = {0.f, 0.f, 0.f, 0.f};
#pragma unroll
      for (int s = 0; s < 4; ++s) S = mfma16(Kt[s], Q[v][s], S);
      const float x0 = fmaf(S[0], CS, bias0), x1 = fmaf(S[1], CS, bias1);
      const float mx = xrow_max(fmaxf(x0, x1));
      const float p0 = __builtin_amdgcn_exp2f(x0 - mx), p1 = __builtin_amdgcn_exp2f(x1 - mx);
      m[v] = mx;
      l[v] = p0 + p1;
      P[v] = u32x4{pack_bf16(p0, p1), 0u, 0u, 0u};
    }
#pragma unroll
    for (int e = 0; e < 8; ++e) {
      const unsigned sel = (e & 1) ? 0x07060302u : 0x05040100u;
      const u32x4 A = {__builtin_amdgcn_perm(Vt[1][e >> 1], Vt[0][e >> 1], sel), 0u, 0u, 0u};
#pragma unroll
      for (int v = 0; v < 2; ++v) O[v][e] = mfma16(A, P[v], O[v][e]);
    }
  }

  int toff = toff0;
#pragma unroll 1
  for (int t = 0; t < NT; ++t) {
    toff += tstep;
    f32x4 S[2][2];
#pragma unroll
    for (int u = 0; u < 2; ++u)
#pragma unroll
      for (int v = 0; v < 2; ++v) {
        S[u][v] = f32x4{0.f, 0.f, 0.f, 0.f};
#pragma unroll
        for (int s = 0; s < 4; ++s) S[u][v] = mfma16(K[u][s], Q[v][s], S[u][v]);
      }
    __builtin_amdgcn_sched_barrier(0);
    LOADK(toff);
    __builtin_amdgcn_sched_barrier(0);
    u32x4 P[2];
#pragma unroll
    for (int v = 0; v < 2; ++v) {
      float mx = fmaxf(fmaxf(fmaxf(S[0][v][0], S[0][v][1]), fmaxf(S[0][v][2], S[0][v][3])),
                       fmaxf(fmaxf(S[1][v][0], S[1][v][1]), fmaxf(S[1][v][2], S[1][v][3])));
      mx = xrow_max(mx) * CS;
      const float mn = fmaxf(m[v], mx);
      const float alpha = __builtin_amdgcn_exp2f(m[v] - mn);
      m[v] = mn;
      float p[8];
#pragma unroll
      for (int u = 0; u < 2; ++u)
#pragma unroll
        for (int i = 0; i < 4; ++i) p[4 * u + i] = __builtin_amdgcn_exp2f(fmaf(S[u][v][i], CS, -mn));
      l[v] = fmaf(l[v], alpha, ((p[0] + p[1]) + (p[2] + p[3])) + ((p[4] + p[5]) + (p[6] + p[7])));
      P[v] = u32x4{pack_bf16(p[0], p[1]), pack_bf16(p[2], p[3]), pack_bf16(p[4], p[5]), pack_bf16(p[6], p[7])};
#pragma unroll
      for (int e = 0; e < 8; ++e) O[v][e] *= alpha;
    }
#pragma unroll
    for (int e = 0; e < 8; ++e) {
      const unsigned sel = (e & 1) ? 0x07060302u : 0x05040100u;
      const int w = e >> 1;
      const u32x4 A = {__builtin_amdgcn_perm(V[1][w], V[0][w], sel), __builtin_amdgcn_perm(V[3][w], V[2][w], sel),
                       __builtin_amdgcn_perm(V[5][w], V[4][w], sel), __builtin_amdgcn_perm(V[7][w], V[6][w], sel)};
#pragma unroll
      for (int v = 0; v < 2; ++v) O[v][e] = mfma16(A, P[v], O[v][e]);
    }
    __builtin_amdgcn_sched_barrier(0);
    LOADV(toff);
    __builtin_amdgcn_sched_barrier(0);
  }
#undef LOADK
#undef LOADV

#pragma unroll
  for (int v = 0; v < 2; ++v)
#pragma unroll
    for (int e = 0; e < 8; ++e)
      *reinterpret_cast<f32x4*>(lds + wave * W_STRIDE + (v * 8 + e) * E_STRIDE + lane * 16) = O[v][e];
  {
    const float l0 = xrow_sum(l[0]), l1 = xrow_sum(l[1]);
    if (g < 2) {
      float2 ml = g == 0 ? make_float2(m[0], l0) : make_float2(m[1], l1);
      *reinterpret_cast<float2*>(lds + ML_OFF + (wave * M_ + 16 * g + c) * 8) = ml;
    }
  }
  __syncthreads();
  {
    const int v = wave >> 2, gg = wave & 3, cc = lane >> 2, h = lane & 3;
    const int row = 16 * v + cc;
    float mw[NW], lw[NW];
#pragma unroll
    for (int w = 0; w < NW; ++w) {
      const float2 ml = *reinterpret_cast<const float2*>(lds + ML_OFF + (w * M_ + row) * 8);
      mw[w] = ml.x;
      lw[w] = ml.y;
    }
    float Mx = mw[0];
#pragma unroll
    for (int w = 1; w < NW; ++w) Mx = fmaxf(Mx, mw[w]);
    float L = 0.f;
    f32x4 a0 = {0.f, 0.f, 0.f, 0.f}, a1 = {0.f, 0.f, 0.f, 0.f};
#pragma unroll
    for (int w = 0; w < NW; ++w) {
      const float f = __builtin_amdgcn_exp2f(mw[w] - Mx);
      L = fmaf(f, lw[w], L);
      const char* p = lds + w * W_STRIDE + (v * 8 + 2 * h) * E_STRIDE + (16 * gg + cc) * 16;
      const f32x4 x0 = *reinterpret_cast<const f32x4*>(p);
      const f32x4 x1 = *reinterpret_cast<const f32x4*>(p + E_STRIDE);
      a0 += f * x0;
      a1 += f * x1;
    }
    const float inv = 1.0f / L;
    unsigned r[4];
#pragma unroll
    for (int i = 0; i < 4; ++i) r[i] = pack_bf16(a0[i] * inv, a1[i] * inv);
    {
      const bool odd = h & 1;
      const unsigned s0 = odd ? r[0] : r[1], s1 = odd ? r[2] : r[3];
      const unsigned t0 = __builtin_amdgcn_mov_dpp(s0, 0xB1, 0xf, 0xf, true), t1 = __builtin_amdgcn_mov_dpp(s1, 0xB1, 0xf, 0xf, true);
      if (odd) { r[0] = t0; r[2] = t1; } else { r[1] = t0; r[3] = t1; }
    }
    {
      const bool hi = h & 2;
      const unsigned s0 = hi ? r[0] : r[2], s1 = hi ? r[1] : r[3];
      const unsigned t0 = __builtin_amdgcn_mov_dpp(s0, 0x4E, 0xf, 0xf, true), t1 = __builtin_amdgcn_mov_dpp(s1, 0x4E, 0xf, 0xf, true);
      if (hi) { r[0] = t0; r[1] = t1; } else { r[2] = t0; r[3] = t1; }
    }
    u16* op = out + ((size_t)bh * M_ + row) * D_ + 32 * gg + 8 * h;
    *reinterpret_cast<u32x4*>(op) = u32x4{r[0], r[1], r[2], r[3]};
  }
}
}

extern "C" void kernel_launch(void* const* d_in, const int* in_sizes, int n_in, void* d_out, int out_size, void* d_ws,
                              size_t ws_size, hipStream_t stream) {
  const u16* q = (const u16*)d_in[0];
  const u16* kc = (const u16*)d_in[1];
  const u16* vc = (const u16*)d_in[2];
  const u16* kt = (const u16*)d_in[3];
  const u16* vt = (const u16*)d_in[4];
  const u16* mask = (const u16*)d_in[5];
  tree_attn_kernel<<<dim3(B_ * HKV), dim3(NW * 64), 0, stream>>>(q, kc, vc, kt, vt, mask, (u16*)d_out);
}
```

```cpp
#include <hip/hip_runtime.h>
#include <hip/hip_bf16.h>
#include <stdint.h>

#define KV_AUX 2
#define Q_AUX 2
#define QREG 0
#define STAMPS 0
#define ORDER 0
#define WARM 0
#define CODEPF 0
#define CPF_LINES 3
#define CODE_BYTES 7168
#define SWZ 1

namespace {
constexpr int B_ = 32, HQ = 32, HKV = 8, T_ = 8, C_ = 2048, D_ = 128;
constexpr int G_ = HQ / HKV;
constexpr int M_ = G_ * T_;
constexpr int NW = 4;
constexpr int TK = 32;
constexpr int NT = C_ / (NW * TK);
constexpr int TILE_BYTES = TK * D_ * 2;
static_assert(M_ == 32 && D_ == 128 && NT % 2 == 0, "layout assumes 32 rows x 128 dims");

typedef __attribute__((ext_vector_type(8))) __bf16 bf16x8;
typedef __attribute__((ext_vector_type(4))) float f32x4;
typedef __attribute__((ext_vector_type(4))) unsigned u32x4;
typedef unsigned short u16;

constexpr float LOG2E = 1.4426950408889634f;
constexpr float SCALE = 0.08838834764831845f;
constexpr float CS = SCALE * LOG2E;

__device__ __forceinline__ f32x4 mfma16(u32x4 a, u32x4 b, f32x4 c) {
  return __builtin_amdgcn_mfma_f32_16x16x32_bf16(__builtin_bit_cast(bf16x8, a), __builtin_bit_cast(bf16x8, b), c, 0, 0, 0);
}
__device__ __forceinline__ unsigned pack_bf16(float lo, float hi) {
  typedef __attribute__((ext_vector_type(2))) float f32x2;
  typedef __attribute__((ext_vector_type(2))) __bf16 bf16x2;
  f32x2 v = {lo, hi};
  bf16x2 r = __builtin_convertvector(v, bf16x2);
  return __builtin_bit_cast(unsigned, r);
}
__device__ __forceinline__ float xrow_max(float x) {
  auto s = __builtin_amdgcn_permlane16_swap(__float_as_uint(x), __float_as_uint(x), false, false);
  x = fmaxf(__uint_as_float(s[0]), __uint_as_float(s[1]));
  auto t = __builtin_amdgcn_permlane32_swap(__float_as_uint(x), __float_as_uint(x), false, false);
  return fmaxf(__uint_as_float(t[0]), __uint_as_float(t[1]));
}
__device__ __forceinline__ float xrow_sum(float x) {
  auto s = __builtin_amdgcn_permlane16_swap(__float_as_uint(x), __float_as_uint(x), false, false);
  x = __uint_as_float(s[0]) + __uint_as_float(s[1]);
  auto t = __builtin_amdgcn_permlane32_swap(__float_as_uint(x), __float_as_uint(x), false, false);
  return __uint_as_float(t[0]) + __uint_as_float(t[1]);
}
__device__ __forceinline__ float bf2f(unsigned x) { return __uint_as_float(x << 16); }

constexpr int E_STRIDE = 64 * 16 + 32;
constexpr int W_STRIDE = 16 * E_STRIDE;
constexpr int ML_OFF = NW * W_STRIDE;
constexpr int T_OFF = ML_OFF + NW * M_ * 8;
constexpr int Q_OFF = T_OFF + 5120;
constexpr int TS_OFF = Q_OFF + 8192;
constexpr int LDS_BYTES = 84 * 1024;
static_assert(TS_OFF + NW * 40 * 8 <= LDS_BYTES, "lds");

#define SB() do { asm volatile("" ::: "memory"); __builtin_amdgcn_sched_barrier(0); } while (0)
#define WAIT8(N, X)                                                                                              \
  asm volatile("s_waitcnt vmcnt(" #N ")"                                                                         \
               : "+v"(X[0]), "+v"(X[1]), "+v"(X[2]), "+v"(X[3]), "+v"(X[4]), "+v"(X[5]), "+v"(X[6]), "+v"(X[7]))
#define WAIT8X(N, X) WAIT8(N, X)
#define WAITV_(N) asm volatile("s_waitcnt vmcnt(" #N ")" ::: "memory")
#define WAITV(N) WAITV_(N)
__device__ __forceinline__ u32x4 make_srd(const void* p, unsigned bytes) {
  const uint64_t a = (uint64_t)p;
  return u32x4{(unsigned)a, (unsigned)(a >> 32) & 0xffffu, bytes, 0x00020000u};
}
__device__ __forceinline__ void dma16(u32x4 srd, int voff, int soff, unsigned ldsb) {
  unsigned keep;
  asm volatile("s_mov_b32 %0, m0\n\ts_mov_b32 m0, %4\n\ts_nop 4\n\tbuffer_load_dwordx4 %1, %2, %3 offen nt lds\n\ts_mov_b32 m0, %0"
               : "=&s"(keep) : "v"(voff), "s"(srd), "s"(soff), "s"(ldsb) : "memory");
}
#define TSTAMP2()
#define TSTAMP3()

__global__ __launch_bounds__(NW * 64, 2) void tree_attn_kernel(const u16* q, const u16* kc,
                                                               const u16* vc, const u16* kt,
                                                               const u16* vt, const u16* mask,
                                                               u16* __restrict__ out
                                                               ) {
  extern __shared__ __attribute__((aligned(16))) char lds[];
  const int bh = (blockIdx.x & 7) * 32 + (blockIdx.x >> 3);
  const int tid = threadIdx.x, lane = tid & 63;
  const int wave = __builtin_amdgcn_readfirstlane(tid >> 6);
  const int g = lane >> 4, c = lane & 15;

  const size_t base = (size_t)bh * C_ * D_;
  const int nrec = C_ * D_ * 2;
  const int toff0 = wave * TILE_BYTES, tstep = NW * TILE_BYTES;
  const __amdgpu_buffer_rsrc_t krs = __builtin_amdgcn_make_buffer_rsrc((void*)(kc + base), 0, nrec, 0x00020000);
  const __amdgpu_buffer_rsrc_t vrs = __builtin_amdgcn_make_buffer_rsrc((void*)(vc + base), 0, nrec, 0x00020000);
  const int kap = 4 * (c & 3) + (c >> 2);
  const int kvo = kap * 256 + g * 16;
  const int vvo = lane * 16;

  u32x4 KA[8], KB[8], VA[8], VB[8];
#define LOADK(K, off)                                                                                        \
  do {                                                                                                       \
    _Pragma("unroll") for (int j = 0; j < 8; ++j)                                                           \
        K[j] = __builtin_amdgcn_raw_buffer_load_b128(krs, kvo + (j & 3) * 64, (off) + (j >> 2) * 4096, KV_AUX); \
  } while (0)
#define LOADV(V, off)                                                                                        \
  do {                                                                                                       \
    _Pragma("unroll") for (int j = 0; j < 8; ++j)                                                           \
        V[j] = __builtin_amdgcn_raw_buffer_load_b128(vrs, vvo + (j & 3) * 1024, (off) + (j >> 2) * 4096, KV_AUX); \
  } while (0)

  const unsigned lds0 = (unsigned)(uintptr_t)(__attribute__((address_space(3))) char*)lds;
  const u32x4 qsrd = make_srd(q + (size_t)bh * M_ * D_, M_ * D_ * 2);
#define LOADQ()                                                                                                  \
  do {                                                                                                           \
    const int vo = g * 256 + ((c ^ (4 * wave + g)) << 4);                                                        \
    dma16(qsrd, vo, wave * 1024, lds0 + Q_OFF + wave * 1024);                                                    \
    dma16(qsrd, vo, wave * 1024 + 4096, lds0 + Q_OFF + wave * 1024 + 4096);                                      \
  } while (0)
#define LOADT()                                                                                                  \
  do {                                                                                                           \
    const u32x4 tsrd = make_srd((wave < 2 ? kt : vt) + (size_t)bh * T_ * D_, T_ * D_ * 2);                       \
    const u32x4 msrd = make_srd(mask + (size_t)(bh / HKV) * T_ * T_, T_ * T_ * 2);                               \
    dma16(tsrd, vvo, (wave & 1) * 1024, lds0 + T_OFF + wave * 1024);                                             \
    if (wave == 0) dma16(msrd, vvo, 0, lds0 + T_OFF + 4096);                                                     \
  } while (0)
  LOADT();
  LOADQ();
  SB();
  LOADK(KA, toff0);
  LOADV(VA, toff0);
  SB();
  TSTAMP2();
  int toff = toff0;
  WAITV(16);
  __builtin_amdgcn_s_barrier();
  SB();
  const char* qb0 = lds + Q_OFF + c * 256;
#define QF(v, s) (*reinterpret_cast<const u32x4*>(qb0 + (((4 * (s) + g) ^ c) << 4) + (v) * 4096))

  float m[2] = {-INFINITY, -INFINITY}, l[2] = {0.f, 0.f};
  f32x4 O[2][8];
#pragma unroll
  for (int v = 0; v < 2; ++v)
#pragma unroll
    for (int e = 0; e < 8; ++e) O[v][e] = f32x4{0.f, 0.f, 0.f, 0.f};

  if (wave == NW - 1) {
    const char* tb = lds + T_OFF;
    const float bias0 = (1.0f - bf2f(*(const u16*)(tb + 4096 + (c & 7) * 16 + g * 2))) * -1000000000.0f * LOG2E;
    const float bias1 = (1.0f - bf2f(*(const u16*)(tb + 4096 + (c & 7) * 16 + g * 2 + 8))) * -1000000000.0f * LOG2E;
    u32x4 Pt[2];
#pragma unroll
    for (int v = 0; v < 2; ++v) {
      f32x4 S = {0.f, 0.f, 0.f, 0.f};
#pragma unroll
      for (int s = 0; s < 4; ++s) S = mfma16(*reinterpret_cast<const u32x4*>(tb + (kap & 7) * 256 + g * 16 + s * 64), QF(v, s), S);
      const float x0 = fmaf(S[0], CS, bias0), x1 = fmaf(S[1], CS, bias1);
      const float mn = xrow_max(fmaxf(x0, x1));
      const float p0 = __builtin_amdgcn_exp2f(x0 - mn), p1 = __builtin_amdgcn_exp2f(x1 - mn);
      m[v] = mn;
      l[v] = p0 + p1;
      Pt[v] = u32x4{pack_bf16(p0, p1), 0u, 0u, 0u};
    }
    const u32x4 Vt0 = *reinterpret_cast<const u32x4*>(tb + 2048 + vvo), Vt1 = *reinterpret_cast<const u32x4*>(tb + 3072 + vvo);
#pragma unroll
    for (int e = 0; e < 8; ++e) {
      const unsigned sel = (e & 1) ? 0x07060302u : 0x05040100u;
      const u32x4 A = {__builtin_amdgcn_perm(Vt1[e >> 1], Vt0[e >> 1], sel), 0u, 0u, 0u};
#pragma unroll
      for (int v = 0; v < 2; ++v) O[v][e] = mfma16(A, Pt[v], O[v][e]);
    }
  }

#define TILE_ABC(K, KN, V, VN)                                                                                  \
    toff += tstep;                                                                                              \
    SB();                                                                                                       \
    WAIT8(8, K);                                                   \
    SB();                                                                                                       \
    LOADK(KN, toff);                               \
    SB();                                                                                                       \
    TSTAMP2();                                                                                                  \
    {                                                                                                           \
    f32x4 S[2][2];                                                                                              \
    _Pragma("unroll") for (int v = 0; v < 2; ++v) {                                                            \
      S[0][v] = f32x4{0.f, 0.f, 0.f, 0.f};                                                                      \
      S[1][v] = f32x4{0.f, 0.f, 0.f, 0.f};                                                                      \
      _Pragma("unroll") for (int s = 0; s < 4; ++s) {                                                          \
        const u32x4 qf = QF(v, s);                                                                              \
        S[0][v] = mfma16(K[s], qf, S[0][v]);                                                                    \
        S[1][v] = mfma16(K[4 + s], qf, S[1][v]);                                                                \
      }                                                                                                         \
    }                                                                                                           \
    _Pragma("unroll") for (int v = 0; v < 2; ++v) {                                                            \
      float mx = fmaxf(fmaxf(fmaxf(S[0][v][0], S[0][v][1]), fmaxf(S[0][v][2], S[0][v][3])),                     \
                       fmaxf(fmaxf(S[1][v][0], S[1][v][1]), fmaxf(S[1][v][2], S[1][v][3])));                    \
      mx = xrow_max(mx) * CS;                                                                                   \
      const float mn = fmaxf(m[v], mx);                                                                         \
      const float alpha = __builtin_amdgcn_exp2f(m[v] - mn);                                                    \
      m[v] = mn;                                                                                                \
      float p[8];                                                                                               \
      _Pragma("unroll") for (int u = 0; u < 2; ++u) _Pragma("unroll") for (int i = 0; i < 4; ++i)              \
          p[4 * u + i] = __builtin_amdgcn_exp2f(fmaf(S[u][v][i], CS, -mn));                                     \
      l[v] = fmaf(l[v], alpha, ((p[0] + p[1]) + (p[2] + p[3])) + ((p[4] + p[5]) + (p[6] + p[7])));              \
      P[v] = u32x4{pack_bf16(p[0], p[1]), pack_bf16(p[2], p[3]), pack_bf16(p[4], p[5]), pack_bf16(p[6], p[7])}; \
      _Pragma("unroll") for (int e = 0; e < 8; ++e) O[v][e] *= alpha;                                           \
    }                                                                                                           \
    }                                                                                                           \
    SB();                                                                                                       \
    WAIT8(8, V);                                                 \
    SB();                                                                                                       \
    LOADV(VN, toff);                                                                                            \
    SB();                                                                                                       \
    TSTAMP2();
#define TILE_D(V)                                                                                               \
    _Pragma("unroll") for (int e = 0; e < 8; ++e) {                                                            \
      const unsigned sel = (e & 1) ? 0x07060302u : 0x05040100u;                                                 \
      const int w = e >> 1;                                                                                     \
      const u32x4 A = {__builtin_amdgcn_perm(V[1][w], V[0][w], sel), __builtin_amdgcn_perm(V[3][w], V[2][w], sel), \
                       __builtin_amdgcn_perm(V[5][w], V[4][w], sel), __builtin_amdgcn_perm(V[7][w], V[6][w], sel)}; \
      _Pragma("unroll") for (int v = 0; v < 2; ++v) O[v][e] = mfma16(A, P[v], O[v][e]);                         \
    }

  u32x4 P[2];
#pragma unroll 1
  for (int t = 0; t < NT; t += 2) {
    TILE_ABC(KA, KB, VA, VB);
    TILE_D(VA);
    TILE_ABC(KB, KA, VB, VA);
    TILE_D(VB);
  }
#undef LOADK
#undef LOADV
  TSTAMP3();

  {
    const float l0 = xrow_sum(l[0]), l1 = xrow_sum(l[1]);
    if (g < 2) {
      float2 ml = g == 0 ? make_float2(m[0], l0) : make_float2(m[1], l1);
      *reinterpret_cast<float2*>(lds + ML_OFF + (wave * M_ + 16 * g + c) * 8) = ml;
    }
  }
#pragma unroll
  for (int v = 0; v < 2; ++v)
#pragma unroll
    for (int e = 0; e < 8; ++e)
      *reinterpret_cast<f32x4*>(lds + wave * W_STRIDE + (v * 8 + e) * E_STRIDE + lane * 16) = O[v][e];
  TSTAMP3();
  asm volatile("s_waitcnt lgkmcnt(0)" ::: "memory");
  __builtin_amdgcn_s_barrier();
  asm volatile("" ::: "memory");
  TSTAMP3();
  {
    const int v = wave >> 1, gg0 = (wave & 1) * 2, cc = lane >> 2, h = lane & 3;
    const int row = 16 * v + cc;
    float2 ml[NW];
    f32x4 x[NW][2][2];
#pragma unroll
    for (int w = 0; w < NW; ++w) ml[w] = *reinterpret_cast<const float2*>(lds + ML_OFF + (w * M_ + row) * 8);
    const char* p = lds + (v * 8 + 2 * h) * E_STRIDE + (16 * gg0 + cc) * 16;
#pragma unroll
    for (int w = 0; w < NW; ++w)
#pragma unroll
      for (int r2 = 0; r2 < 2; ++r2)
#pragma unroll
        for (int b = 0; b < 2; ++b) x[w][r2][b] = *reinterpret_cast<const f32x4*>(p + w * W_STRIDE + r2 * 256 + b * E_STRIDE);
    SB();
    const float Mx = fmaxf(fmaxf(ml[0].x, ml[1].x), fmaxf(ml[2].x, ml[3].x));
    float f[NW], L = 0.f;
#pragma unroll
    for (int w = 0; w < NW; ++w) { f[w] = __builtin_amdgcn_exp2f(ml[w].x - Mx); L = fmaf(f[w], ml[w].y, L); }
    const float inv = 1.0f / L;
#pragma unroll
    for (int w = 0; w < NW; ++w) f[w] *= inv;
#pragma unroll
    for (int r2 = 0; r2 < 2; ++r2) {
      f32x4 a0 = f[0] * x[0][r2][0], a1 = f[0] * x[0][r2][1];
#pragma unroll
      for (int w = 1; w < NW; ++w) { a0 += f[w] * x[w][r2][0]; a1 += f[w] * x[w][r2][1]; }
      unsigned r[4];
#pragma unroll
      for (int i = 0; i < 4; ++i) r[i] = pack_bf16(a0[i], a1[i]);
      {
        const bool odd = h & 1;
        const unsigned s0 = odd ? r[0] : r[1], s1 = odd ? r[2] : r[3];
        const unsigned t0 = __builtin_amdgcn_mov_dpp(s0, 0xB1, 0xf, 0xf, true), t1 = __builtin_amdgcn_mov_dpp(s1, 0xB1, 0xf, 0xf, true);
        if (odd) { r[0] = t0; r[2] = t1; } else { r[1] = t0; r[3] = t1; }
      }
      {
        const bool hi = h & 2;
        const unsigned s0 = hi ? r[0] : r[2], s1 = hi ? r[1] : r[3];
        const unsigned t0 = __builtin_amdgcn_mov_dpp(s0, 0x4E, 0xf, 0xf, true), t1 = __builtin_amdgcn_mov_dpp(s1, 0x4E, 0xf, 0xf, true);
        if (hi) { r[0] = t0; r[1] = t1; } else { r[2] = t0; r[3] = t1; }
      }
      u16* op = out + ((size_t)bh * M_ + row) * D_ + 32 * (gg0 + r2) + 8 * h;
      *reinterpret_cast<u32x4*>(op) = u32x4{r[0], r[1], r[2], r[3]};
    }
  }
  TSTAMP3();
  asm volatile("" :: "v"(KA[0]), "v"(KA[1]), "v"(KA[2]), "v"(KA[3]), "v"(KA[4]), "v"(KA[5]), "v"(KA[6]), "v"(KA[7]),
                     "v"(VA[0]), "v"(VA[1]), "v"(VA[2]), "v"(VA[3]), "v"(VA[4]), "v"(VA[5]), "v"(VA[6]), "v"(VA[7]));
}
}

extern "C" void kernel_launch(void* const* d_in, const int* in_sizes, int n_in, void* d_out, int out_size, void* d_ws,
                              size_t ws_size, hipStream_t stream) {
  const u16* q = (const u16*)d_in[0];
  const u16* kc = (const u16*)d_in[1];
  const u16* vc = (const u16*)d_in[2];
  const u16* kt = (const u16*)d_in[3];
  const u16* vt = (const u16*)d_in[4];
  const u16* mask = (const u16*)d_in[5];
  (void)hipFuncSetAttribute((const void*)tree_attn_kernel, hipFuncAttributeMaxDynamicSharedMemorySize, LDS_BYTES);
  tree_attn_kernel<<<dim3(B_ * HKV), dim3(NW * 64), LDS_BYTES, stream>>>(q, kc, vc, kt, vt, mask, (u16*)d_out
  );
}
```

```cpp
#include <hip/hip_runtime.h>
#include <hip/hip_bf16.h>
#include <stdint.h>

#define WINL 22
#define PACE 3
#define PACE_STR "s_nop 11\n\t"
#define NOCOMP 0
#define POLL_SLEEP
#define DRY 0
#define OUTWARM 0
#define O_AUX 2
#define ILV 1
#define STAMPS 0

namespace {
constexpr int B_ = 32, HQ = 32, HKV = 8, T_ = 8, C_ = 2048, D_ = 128;
constexpr int G_ = HQ / HKV;
constexpr int M_ = G_ * T_;
constexpr int NTILE = C_ / 32;
static_assert(M_ == 32 && D_ == 128 && T_ == 8 && WINL >= 17 && WINL <= 24, "layout assumptions");

typedef __attribute__((ext_vector_type(8))) __bf16 bf16x8;
typedef __attribute__((ext_vector_type(4))) float f32x4;
typedef __attribute__((ext_vector_type(4))) unsigned u32x4;
typedef __attribute__((ext_vector_type(2))) unsigned u32x2;
typedef __attribute__((ext_vector_type(4))) short s16x4;
typedef unsigned short u16;
#define LAS __attribute__((address_space(3)))

constexpr float LOG2E = 1.4426950408889634f;
constexpr float SCALE = 0.08838834764831845f;
constexpr float CS = SCALE * LOG2E;

constexpr int VR_OFF = 65536, CTL_OFF = 131072, TS_OFF = CTL_OFF + 64;
constexpr int LDS_BYTES = CTL_OFF + 64 + (STAMPS ? 3 * 40 * 8 : 0);
constexpr int NPIECE = 4 + NTILE * 16;
constexpr unsigned SPIN_CAP = 1u << 24;

__device__ __forceinline__ f32x4 mfma16(u32x4 a, u32x4 b, f32x4 c) {
  return __builtin_amdgcn_mfma_f32_16x16x32_bf16(__builtin_bit_cast(bf16x8, a), __builtin_bit_cast(bf16x8, b), c, 0, 0, 0);
}
__device__ __forceinline__ unsigned pack_bf16(float lo, float hi) {
  typedef __attribute__((ext_vector_type(2))) float f32x2;
  typedef __attribute__((ext_vector_type(2))) __bf16 bf16x2;
  f32x2 v = {lo, hi};
  bf16x2 r = __builtin_convertvector(v, bf16x2);
  return __builtin_bit_cast(unsigned, r);
}
__device__ __forceinline__ float xrow_max(float x) {
  auto s = __builtin_amdgcn_permlane16_swap(__float_as_uint(x), __float_as_uint(x), false, false);
  x = fmaxf(__uint_as_float(s[0]), __uint_as_float(s[1]));
  auto t = __builtin_amdgcn_permlane32_swap(__float_as_uint(x), __float_as_uint(x), false, false);
  return fmaxf(__uint_as_float(t[0]), __uint_as_float(t[1]));
}
__device__ __forceinline__ float xrow_sum(float x) {
  auto s = __builtin_amdgcn_permlane16_swap(__float_as_uint(x), __float_as_uint(x), false, false);
  x = __uint_as_float(s[0]) + __uint_as_float(s[1]);
  auto t = __builtin_amdgcn_permlane32_swap(__float_as_uint(x), __float_as_uint(x), false, false);
  return __uint_as_float(t[0]) + __uint_as_float(t[1]);
}
__device__ __forceinline__ u32x4 make_srd(const void* p, unsigned bytes) {
  const uint64_t a = (uint64_t)p;
  return u32x4{(unsigned)a, (unsigned)(a >> 32) & 0xffffu, bytes, 0x00020000u};
}
#define STR_(x) #x
#define STR(x) STR_(x)
#define SET_M0(x) asm volatile("s_mov_b32 m0, %0\n\ts_nop 0" :: "s"(x) : "memory")
#define DMA(srd, voff, soff, IMM)                                                                                  \
  asm volatile("buffer_load_dwordx4 %0, %1, %2 offen offset:" #IMM " nt lds\n\t" PACE_STR "s_waitcnt vmcnt(" STR(WINL) ")"     \
               :: "v"(voff), "s"(srd), "s"(soff) : "memory")
#define MEMFENCE() asm volatile("" ::: "memory")

__global__ __launch_bounds__(192, 2) void tree_attn_kernel(const u16* q, const u16* kc, const u16* vc, const u16* kt, const u16* vt,
                                                            const u16* mask, u16* out
) {
  extern __shared__ __attribute__((aligned(16))) char lds_generic[];
  LAS char* lds = (LAS char*)lds_generic;
#define TSTAMP2()
#define TSTAMP3()
  const int bh = (blockIdx.x & 7) * 32 + (blockIdx.x >> 3);
  const int lane = threadIdx.x & 63;
  const int wave = __builtin_amdgcn_readfirstlane(threadIdx.x >> 6);
  const int g = lane >> 4, c = lane & 15;
  volatile LAS unsigned* FULL = (volatile LAS unsigned*)(lds + CTL_OFF);
  volatile LAS unsigned* DONE = (volatile LAS unsigned*)(lds + CTL_OFF + 8);

  if (__builtin_expect(wave == 0, 1)) {
    const unsigned lds0 = (unsigned)(uintptr_t)lds;
    const size_t base = (size_t)bh * C_ * D_;
    const u32x4 krs = make_srd(kc + base, C_ * D_ * 2), vrs = make_srd(vc + base, C_ * D_ * 2);
    const int kv0 = g * 256 + ((c ^ g) << 4), kv1 = kv0 ^ 64, kv2 = kv0 ^ 128, kv3 = kv0 ^ 192;
    const int vv0 = g * 256 + ((c ^ (g << 2)) << 4);
#define KBATCH(go, P, i)                                                                                             \
    SET_M0(lds0 + (((go) + 8192) & 65535));                                                                          \
    DMA(krs, kv0, go, 0); P(i + 1) DMA(krs, kv1, go, 1024); P(i + 2) DMA(krs, kv2, go, 2048); P(i + 3) DMA(krs, kv3, go, 3072); P(i + 4)
#define VBATCH(go, P, i)                                                                                             \
    SET_M0(lds0 + VR_OFF + (((go) + 8192) & 65535));                                                                 \
    DMA(vrs, vv0, go, 0); P(i + 1) DMA(vrs, vv0, go, 1024); P(i + 2) DMA(vrs, vv0, go, 2048); P(i + 3) DMA(vrs, vv0, go, 3072); P(i + 4)
#define NOPUB(i)
#define PUB(i) if ((i) == ((WINL - 1) & 15) + 1) *FULL = 20 + 16 * t + (i) - WINL;
#define ROUND(so, P) KBATCH(so + 16384, P, 0) VBATCH(so, P, 4) KBATCH(so + 20480, P, 8) VBATCH(so + 4096, P, 12)
#define WAIT_DONE(need)                                                                                              \
    if (__builtin_expect((int)dmin < (need), 0)) {                                                                   \
      _Pragma("unroll 1") for (unsigned sp = 0; sp < SPIN_CAP; ++sp) {                                              \
        dmin = __builtin_amdgcn_readfirstlane(min(DONE[0], DONE[1]));                                                \
        if ((int)dmin >= (need)) break;                                                                              \
        __builtin_amdgcn_s_sleep(1);                                                                                 \
      }                                                                                                              \
      MEMFENCE();                                                                                                    \
    }
    KBATCH(0, NOPUB, 0)
    KBATCH(4096, NOPUB, 0)
    {
      const u32x4 ktrs = make_srd(kt + (size_t)bh * T_ * D_, T_ * D_ * 2), vtrs = make_srd(vt + (size_t)bh * T_ * D_, T_ * D_ * 2);
      SET_M0(lds0);
      DMA(ktrs, kv0, 0, 0); DMA(ktrs, kv1, 0, 1024);
      SET_M0(lds0 + VR_OFF);
      DMA(vtrs, vv0, 0, 0); DMA(vtrs, vv0, 0, 1024);
    }
    KBATCH(8192, NOPUB, 0)
    KBATCH(12288, NOPUB, 0)
    TSTAMP2();
    __builtin_amdgcn_s_barrier();
    MEMFENCE();
    TSTAMP2();
#define PUB0(i) if ((i) == WINL - 8) *FULL = 12;
    ROUND(0, PUB0)
    unsigned dmin = 0;
#pragma unroll 1
    for (int t = 1; t < NTILE - 2; ++t) {
      WAIT_DONE(t - 4)
      const unsigned d0 = DONE[0], d1 = DONE[1];
      const int so = t * 8192;
      ROUND(so, PUB)
      dmin = __builtin_amdgcn_readfirstlane(min(d0, d1));
      if ((t & 1) == 1) TSTAMP2();
    }
    {
      const int t = NTILE - 2;
      WAIT_DONE(NTILE - 6)
      VBATCH(t * 8192, PUB, 0)
      VBATCH(t * 8192 + 4096, PUB, 4)
      VBATCH(t * 8192 + 8192, PUB, 8)
      VBATCH(t * 8192 + 12288, PUB, 12)
    }
#define DRAIN(n) asm volatile("s_waitcnt vmcnt(" #n ")" ::: "memory"); *FULL = NPIECE - n;
    DRAIN(16) DRAIN(8) DRAIN(0)
  } else {
    const int v = wave - 1;
    u32x4 Q[4];
    {
      const __amdgpu_buffer_rsrc_t qrs = __builtin_amdgcn_make_buffer_rsrc((void*)(q + ((size_t)bh * M_ + 16 * v) * D_), 0, 16 * D_ * 2, 0x00020000);
#pragma unroll
      for (int s = 0; s < 4; ++s) Q[s] = __builtin_amdgcn_raw_buffer_load_b128(qrs, c * 256 + g * 16 + s * 64, 0, 2);
    }
    const __amdgpu_buffer_rsrc_t mrs = __builtin_amdgcn_make_buffer_rsrc((void*)(mask + (size_t)(bh / HKV) * T_ * T_), 0, T_ * T_ * 2, 0x00020000);
    u32x2 mk = __builtin_amdgcn_raw_buffer_load_b64(mrs, (c & 7) * 16 + (g & 1) * 8, 0, 2);
#pragma unroll
    for (int j = 0; j < 6; ++j) *(LAS u32x4*)(lds + v * VR_OFF + 2048 + j * 1024 + lane * 16) = u32x4{0u, 0u, 0u, 0u};
    if (v == 0 && lane < 4) FULL[lane] = 0u;
    asm volatile("s_waitcnt lgkmcnt(0)" ::: "memory");
    __builtin_amdgcn_s_barrier();
    MEMFENCE();
    int ka[4], va[4];
#pragma unroll
    for (int s = 0; s < 4; ++s) ka[s] = c * 256 + (((4 * s + g) ^ c) << 4);
#pragma unroll
    for (int h = 0; h < 4; ++h) va[h] = VR_OFF + (4 * g + (c >> 2)) * 256 + ((4 * (h ^ (c >> 2)) + (c & 3)) << 4);

#define WAIT_FULL(need)                                                        \
  do {                                                                         \
    if (__builtin_amdgcn_readfirstlane(fpre) < (unsigned)(need)) {             \
      for (unsigned sp = 0; sp < SPIN_CAP; ++sp) {                             \
        if (__builtin_amdgcn_readfirstlane(*FULL) >= (unsigned)(need)) break;  \
        POLL_SLEEP                                                             \
      }                                                                        \
    }                                                                          \
    MEMFENCE();                                                                \
  } while (0)
#define VREAD(so)                                                                                                    \
    _Pragma("unroll") for (int e = 0; e < 8; ++e) _Pragma("unroll") for (int u = 0; u < 2; ++u)                     \
        vf[e][u] = __builtin_bit_cast(u32x2, __builtin_amdgcn_ds_read_tr16_b64_v4i16(                                \
            (LAS s16x4*)(lds + (so) + va[e >> 1] + (e & 1) * 8 + u * 4096)));
#define PVMMA(e) O[e] = mfma16(u32x4{vf[e][0][0], vf[e][0][1], vf[e][1][0], vf[e][1][1]}, P, O[e])

    u32x2 vf[8][2];
    int pass = DRY ? 0 : 1;
#pragma unroll 1
    for (;;) {
    asm volatile("" : "+s"(pass));
    unsigned fpre = 0;
    float m = -INFINITY, l = 0.f;
    f32x4 O[8];
#pragma unroll
    for (int e = 0; e < 8; ++e) O[e] = f32x4{0.f, 0.f, 0.f, 0.f};
    u32x4 P = {0u, 0u, 0u, 0u}, Pn = {0u, 0u, 0u, 0u};
    float alpha_p = 1.f;
    float p[8] = {0.f, 0.f, 0.f, 0.f, 0.f, 0.f, 0.f, 0.f};
    if (pass) {
    asm volatile("" : "+v"(mk));
    f32x4 Ci[2];
    {
      const float NB = -1000000000.0f / SCALE;
      const float b0 = (1.0f - __uint_as_float(mk[0] << 16)) * NB, b1 = (1.0f - __uint_as_float(mk[0] & 0xffff0000u)) * NB;
      const float b2 = (1.0f - __uint_as_float(mk[1] << 16)) * NB, b3 = (1.0f - __uint_as_float(mk[1] & 0xffff0000u)) * NB;
      const float ninf = -INFINITY;
      Ci[0] = g < 2 ? f32x4{b0, b1, b2, b3} : f32x4{ninf, ninf, ninf, ninf};
      Ci[1] = f32x4{ninf, ninf, ninf, ninf};
    }
#pragma unroll 1
    for (int tau = 0; tau <= NTILE; ++tau) {
      WAIT_FULL(max(16 * tau - 12, 12));
      f32x4 S[2];
      {
        u32x4 kf[2][4];
        const int so = (tau & 7) * 8192;
#pragma unroll
        for (int s = 0; s < 4; ++s)
#pragma unroll
          for (int u = 0; u < 2; ++u) kf[u][s] = *(const LAS u32x4*)(lds + so + ka[s] + u * 4096);
        __builtin_amdgcn_sched_barrier(0);
        VREAD((max(tau - 2, 0) & 7) * 8192);
        __builtin_amdgcn_sched_barrier(0);
        S[0] = Ci[0];
        S[1] = Ci[1];
#pragma unroll
        for (int s = 0; s < 4; ++s)
#pragma unroll
          for (int u = 0; u < 2; ++u) S[u] = mfma16(kf[u][s], Q[s], S[u]);
        Ci[0] = Ci[1] = f32x4{0.f, 0.f, 0.f, 0.f};
        __builtin_amdgcn_sched_barrier(0);
#pragma unroll
        for (int e = 0; e < 8; ++e) PVMMA(e);
      }
      asm volatile("s_waitcnt lgkmcnt(0)" ::: "memory");
      DONE[v] = tau;
      fpre = *FULL;
      if ((tau & 1) == 0) TSTAMP2();
      float x[8];
#pragma unroll
      for (int u = 0; u < 2; ++u)
#pragma unroll
        for (int i = 0; i < 4; ++i) {
          x[4 * u + i] = S[u][i] * CS;
          asm("" : "+v"(x[4 * u + i]));
        }
      const float mx = xrow_max(fmaxf(fmaxf(fmaxf(x[0], x[1]), fmaxf(x[2], x[3])), fmaxf(fmaxf(x[4], x[5]), fmaxf(x[6], x[7]))));
      const float mn = fmaxf(m, mx);
      const float alpha = __builtin_amdgcn_exp2f(m - mn);
      m = mn;
#pragma unroll
      for (int j = 0; j < 8; ++j) p[j] = __builtin_amdgcn_exp2f(x[j] - mn);
      l = fmaf(l, alpha, ((p[0] + p[1]) + (p[2] + p[3])) + ((p[4] + p[5]) + (p[6] + p[7])));
      P = Pn;
      Pn = u32x4{pack_bf16(p[0], p[1]), pack_bf16(p[2], p[3]), pack_bf16(p[4], p[5]), pack_bf16(p[6], p[7])};
#pragma unroll
      for (int e = 0; e < 8; ++e) O[e] *= alpha_p;
      alpha_p = alpha;
    }
    }
    const float inv = __builtin_amdgcn_rcpf(xrow_sum(l));
    Pn = u32x4{pack_bf16(p[0] * inv, p[1] * inv), pack_bf16(p[2] * inv, p[3] * inv), pack_bf16(p[4] * inv, p[5] * inv), pack_bf16(p[6] * inv, p[7] * inv)};
    alpha_p *= inv;
    const __amdgpu_buffer_rsrc_t ors = __builtin_amdgcn_make_buffer_rsrc((void*)(out + ((size_t)bh * M_ + 16 * v) * D_), 0, pass ? 16 * D_ * 2 : 0, 0x00020000);
    asm volatile("" : "+v"(Pn), "+v"(alpha_p));
    WAIT_FULL(pass ? NPIECE - 8 : 0);
    VREAD(((NTILE - 1) & 7) * 8192);
    fpre = *FULL;
    __builtin_amdgcn_sched_barrier(0);
#pragma unroll
    for (int e = 0; e < 8; ++e) PVMMA(e);
    __builtin_amdgcn_sched_barrier(0);
#pragma unroll
    for (int e = 0; e < 8; ++e) O[e] *= alpha_p;
    P = Pn;
    asm volatile("" : "+v"(O[0]), "+v"(O[1]), "+v"(O[2]), "+v"(O[3]), "+v"(O[4]), "+v"(O[5]), "+v"(O[6]), "+v"(O[7]));
    WAIT_FULL(pass ? NPIECE : 0);
    VREAD((NTILE & 7) * 8192);
    __builtin_amdgcn_sched_barrier(0);
#pragma unroll
    for (int e = 0; e < 8; ++e) PVMMA(e);
    __builtin_amdgcn_sched_barrier(0);
#pragma unroll
    for (int e2 = 0; e2 < 4; ++e2) {
      const f32x4 a = O[2 * e2], b = O[2 * e2 + 1];
      __builtin_amdgcn_raw_buffer_store_b128(u32x4{pack_bf16(a[0], a[1]), pack_bf16(a[2], a[3]), pack_bf16(b[0], b[1]), pack_bf16(b[2], b[3])},
                                             ors, c * 256 + g * 16 + e2 * 64, 0, O_AUX);
    }
    if (pass) break;
    pass = 1;
    }
  }
}
}

extern "C" void kernel_launch(void* const* d_in, const int* in_sizes, int n_in, void* d_out, int out_size, void* d_ws,
                              size_t ws_size, hipStream_t stream) {
  const u16* q = (const u16*)d_in[0];
  const u16* kc = (const u16*)d_in[1];
  const u16* vc = (const u16*)d_in[2];
  const u16* kt = (const u16*)d_in[3];
  const u16* vt = (const u16*)d_in[4];
  const u16* mask = (const u16*)d_in[5];
  (void)hipFuncSetAttribute((const void*)tree_attn_kernel, hipFuncAttributeMaxDynamicSharedMemorySize, LDS_BYTES);
  tree_attn_kernel<<<dim3(B_ * HKV), dim3(192), LDS_BYTES, stream>>>(q, kc, vc, kt, vt, mask, (u16*)d_out
  );
}
```

```cpp
#include <hip/hip_runtime.h>
#include <hip/hip_bf16.h>
#include <stdint.h>

#define WINL 20
#define WINB 28

namespace {
constexpr int B_ = 32, HQ = 32, HKV = 8, T_ = 8, C_ = 2048, D_ = 128;
constexpr int G_ = HQ / HKV;
constexpr int M_ = G_ * T_;
constexpr int NWG = B_ * HKV;
constexpr int NTILE = C_ / 32;
constexpr int NPIECE = 4 + NTILE * 16;
constexpr int JA = 2, JB = 16, JC = 22;
constexpr float PMARG = 1.002f;
static_assert(M_ == 32 && D_ == 128 && T_ == 8 && NWG == 256 && WINL >= 17 && WINL <= 24 && WINB <= 32, "layout assumptions");
static_assert(JB % 2 == 0 && JC % 2 == 0 && JC >= JB + 4, "exchange steps");

typedef __attribute__((ext_vector_type(8))) __bf16 bf16x8;
typedef __attribute__((ext_vector_type(4))) float f32x4;
typedef __attribute__((ext_vector_type(4))) unsigned u32x4;
typedef __attribute__((ext_vector_type(2))) unsigned u32x2;
typedef __attribute__((ext_vector_type(4))) short s16x4;
typedef unsigned short u16;
#define LAS __attribute__((address_space(3)))

constexpr float LOG2E = 1.4426950408889634f;
constexpr float SCALE = 0.08838834764831845f;
constexpr float CS = SCALE * LOG2E;

constexpr int VR_OFF = 65536, CTL_OFF = 131072, LDS_BYTES = CTL_OFF + 64;
constexpr unsigned SPIN_CAP = 1u << 18;

__device__ __forceinline__ f32x4 mfma16(u32x4 a, u32x4 b, f32x4 c) {
  return __builtin_amdgcn_mfma_f32_16x16x32_bf16(__builtin_bit_cast(bf16x8, a), __builtin_bit_cast(bf16x8, b), c, 0, 0, 0);
}
__device__ __forceinline__ unsigned pack_bf16(float lo, float hi) {
  typedef __attribute__((ext_vector_type(2))) float f32x2;
  typedef __attribute__((ext_vector_type(2))) __bf16 bf16x2;
  f32x2 v = {lo, hi};
  bf16x2 r = __builtin_convertvector(v, bf16x2);
  return __builtin_bit_cast(unsigned, r);
}
__device__ __forceinline__ float max3(float a, float b, float c) { return fmaxf(fmaxf(a, b), c); }
__device__ __forceinline__ float xrow_max(float x) {
  auto s = __builtin_amdgcn_permlane16_swap(__float_as_uint(x), __float_as_uint(x), false, false);
  x = fmaxf(__uint_as_float(s[0]), __uint_as_float(s[1]));
  auto t = __builtin_amdgcn_permlane32_swap(__float_as_uint(x), __float_as_uint(x), false, false);
  return fmaxf(__uint_as_float(t[0]), __uint_as_float(t[1]));
}
__device__ __forceinline__ float xrow_sum(float x) {
  auto s = __builtin_amdgcn_permlane16_swap(__float_as_uint(x), __float_as_uint(x), false, false);
  x = __uint_as_float(s[0]) + __uint_as_float(s[1]);
  auto t = __builtin_amdgcn_permlane32_swap(__float_as_uint(x), __float_as_uint(x), false, false);
  return __uint_as_float(t[0]) + __uint_as_float(t[1]);
}
__device__ __forceinline__ float wave_sum(float x) {
  x += __builtin_bit_cast(float, __builtin_amdgcn_mov_dpp(__builtin_bit_cast(int, x), 0xB1, 0xf, 0xf, true));
  x += __builtin_bit_cast(float, __builtin_amdgcn_mov_dpp(__builtin_bit_cast(int, x), 0x4E, 0xf, 0xf, true));
  x += __builtin_bit_cast(float, __builtin_amdgcn_mov_dpp(__builtin_bit_cast(int, x), 0x141, 0xf, 0xf, true));
  x += __builtin_bit_cast(float, __builtin_amdgcn_mov_dpp(__builtin_bit_cast(int, x), 0x140, 0xf, 0xf, true));
  return xrow_sum(x);
}
__device__ __forceinline__ u32x4 make_srd(const void* p, unsigned bytes) {
  const uint64_t a = (uint64_t)p;
  return u32x4{(unsigned)a, (unsigned)(a >> 32) & 0xffffu, bytes, 0x00020000u};
}
#define STR_(x) #x
#define STR(x) STR_(x)
#define SET_M0(x) asm volatile("s_mov_b32 m0, %0\n\ts_nop 0" :: "s"(x) : "memory")
#define DMAW(srd, voff, soff, IMM, W)                                                                            \
  asm volatile("buffer_load_dwordx4 %0, %1, %2 offen offset:" #IMM " nt lds\n\ts_waitcnt vmcnt(" STR(W) ")"      \
               :: "v"(voff), "s"(srd), "s"(soff) : "memory")
#define DMA(srd, voff, soff, IMM) DMAW(srd, voff, soff, IMM, WINL)
#define DMAB(srd, voff, soff, IMM) DMAW(srd, voff, soff, IMM, WINB)
#define MEMFENCE() asm volatile("" ::: "memory")
#define SCHEDB() __builtin_amdgcn_sched_barrier(0)

__global__ __launch_bounds__(192, 2) void tree_attn_kernel(const u16* q, const u16* kc, const u16* vc, const u16* kt, const u16* vt,
                                                            const u16* mask, u16* out, unsigned long long* prog) {
  extern __shared__ __attribute__((aligned(16))) char lds_generic[];
  LAS char* lds = (LAS char*)lds_generic;
  const int bh = (blockIdx.x & 7) * 32 + (blockIdx.x >> 3);
  const int lane = threadIdx.x & 63;
  const int wave = __builtin_amdgcn_readfirstlane(threadIdx.x >> 6);
  const int g = lane >> 4, c = lane & 15;
  volatile LAS unsigned* FULL = (volatile LAS unsigned*)(lds + CTL_OFF);
  volatile LAS unsigned* DONE = (volatile LAS unsigned*)(lds + CTL_OFF + 16);
  volatile LAS unsigned* SCHED = (volatile LAS unsigned*)(lds + CTL_OFF + 24);

  if (wave == 0) {
    const unsigned lds0 = (unsigned)(uintptr_t)lds;
    const size_t base = (size_t)bh * C_ * D_;
    const u32x4 krs = make_srd(kc + base, C_ * D_ * 2), vrs = make_srd(vc + base, C_ * D_ * 2);
    const int kv0 = g * 256 + ((c ^ g) << 4), kv1 = kv0 ^ 64, kv2 = kv0 ^ 128, kv3 = kv0 ^ 192;
    const int vv0 = g * 256 + ((c ^ (g << 2)) << 4);
#define KBATCH(D, go, P, i)                                                                                          \
    SET_M0(lds0 + (((go) + 8192) & 65535));                                                                          \
    D(krs, kv0, go, 0); P(i + 1) D(krs, kv1, go, 1024); P(i + 2) D(krs, kv2, go, 2048); P(i + 3) D(krs, kv3, go, 3072); P(i + 4)
#define VBATCH(D, go, P, i)                                                                                          \
    SET_M0(lds0 + VR_OFF + (((go) + 8192) & 65535));                                                                 \
    D(vrs, vv0, go, 0); P(i + 1) D(vrs, vv0, go, 1024); P(i + 2) D(vrs, vv0, go, 2048); P(i + 3) D(vrs, vv0, go, 3072); P(i + 4)
#define NOPUB(i)
#define PUB(i) if ((i) == WINL - 16) *FULL = 4 + 16 * t; if ((i) == WINL - 8 && t == 0) *FULL = 12;
#define PUBB(i) if ((i) == WINB - 16) *FULL = 4 + 16 * t;
#define ROUND(D, so, P) KBATCH(D, so + 16384, P, 0) VBATCH(D, so, P, 4) KBATCH(D, so + 20480, P, 8) VBATCH(D, so + 4096, P, 12)
#define WAIT_DONE(need)                                                                                              \
    if (__builtin_expect((int)dmin < (need), 0)) {                                                                   \
      _Pragma("unroll 1") for (unsigned sp = 0; sp < SPIN_CAP; ++sp) {                                              \
        dmin = __builtin_amdgcn_readfirstlane(min(DONE[0], DONE[1]));                                                \
        if ((int)dmin >= (need)) break;                                                                              \
        __builtin_amdgcn_s_sleep(1);                                                                                 \
      }                                                                                                              \
      MEMFENCE();                                                                                                    \
    }
    {
      const u32x4 ktrs = make_srd(kt + (size_t)bh * T_ * D_, T_ * D_ * 2), vtrs = make_srd(vt + (size_t)bh * T_ * D_, T_ * D_ * 2);
      SET_M0(lds0);
      DMA(ktrs, kv0, 0, 0); DMA(ktrs, kv1, 0, 1024);
      SET_M0(lds0 + VR_OFF);
      DMA(vtrs, vv0, 0, 0); DMA(vtrs, vv0, 0, 1024);
    }
    KBATCH(DMA, 0, NOPUB, 0) KBATCH(DMA, 4096, NOPUB, 0) KBATCH(DMA, 8192, NOPUB, 0) KBATCH(DMA, 12288, NOPUB, 0)
    __builtin_amdgcn_s_barrier();
    MEMFENCE();
    unsigned dmin = 0;
    unsigned sbase = 0, sper = 0;
#pragma unroll 1
    for (int t = 0; t < NTILE - 2; ++t) {
      WAIT_DONE(t - 4)
      const u32x4 cw = *(volatile LAS u32x4*)DONE;
      const int so = t * 8192;
      if (t == JA || t == JB) FULL[t == JA ? 2 : 3] = (unsigned)__builtin_amdgcn_s_memrealtime();
      int lag = 0;
      if (sper) {
        const unsigned due = sbase + t * sper;
        lag = (int)(((unsigned)__builtin_amdgcn_s_memrealtime() << 4) - due);
#pragma unroll 1
        for (int sp = 0; sp < 64 && lag < 0; ++sp) lag = (int)(((unsigned)__builtin_amdgcn_s_memrealtime() << 4) - due);
      }
      if (lag > 80) {
        ROUND(DMAB, so, PUBB)
      } else {
        ROUND(DMA, so, PUB)
      }
      dmin = __builtin_amdgcn_readfirstlane(min(cw[0], cw[1]));
      sbase = __builtin_amdgcn_readfirstlane(cw[2]);
      sper = __builtin_amdgcn_readfirstlane(cw[3]);
    }
    WAIT_DONE(NTILE - 5)
    {
      const int t = NTILE - 2;
      VBATCH(DMA, t * 8192, PUB, 0) VBATCH(DMA, t * 8192 + 4096, PUB, 4) VBATCH(DMA, t * 8192 + 8192, PUB, 8) VBATCH(DMA, t * 8192 + 12288, PUB, 12)
    }
#define DRAIN(n) asm volatile("s_waitcnt vmcnt(" #n ")" ::: "memory"); *FULL = NPIECE - n;
    DRAIN(16) DRAIN(8) DRAIN(0)
  } else {
    const int v = wave - 1;
    u32x4 Q[4];
    {
      const __amdgpu_buffer_rsrc_t qrs = __builtin_amdgcn_make_buffer_rsrc((void*)(q + ((size_t)bh * M_ + 16 * v) * D_), 0, 16 * D_ * 2, 0x00020000);
#pragma unroll
      for (int s = 0; s < 4; ++s) Q[s] = __builtin_amdgcn_raw_buffer_load_b128(qrs, c * 256 + g * 16 + s * 64, 0, 2);
    }
    const __amdgpu_buffer_rsrc_t mrs = __builtin_amdgcn_make_buffer_rsrc((void*)(mask + (size_t)(bh / HKV) * T_ * T_), 0, T_ * T_ * 2, 0x00020000);
    u32x2 mk = __builtin_amdgcn_raw_buffer_load_b64(mrs, (c & 7) * 16 + (g & 1) * 8, 0, 2);
#pragma unroll
    for (int j = 0; j < 3; ++j) *(LAS u32x4*)(lds + VR_OFF + 2048 + (v * 3 + j) * 1024 + lane * 16) = u32x4{0u, 0u, 0u, 0u};
    if (v == 0 && lane < 8) FULL[lane] = 0u;
    asm volatile("s_waitcnt lgkmcnt(0)" ::: "memory");
    __builtin_amdgcn_s_barrier();
    asm volatile("" : "+v"(mk) :: "memory");

    float m = -INFINITY, l = 0.f;
    f32x4 O[8];
#pragma unroll
    for (int e = 0; e < 8; ++e) O[e] = f32x4{0.f, 0.f, 0.f, 0.f};
    u32x4 PA = {0u, 0u, 0u, 0u}, PB = {0u, 0u, 0u, 0u};
    float alpha_p = 1.f;
    float p[8];
    int ka[4], va[4];
#pragma unroll
    for (int s = 0; s < 4; ++s) ka[s] = c * 256 + (((4 * s + g) ^ c) << 4);
#pragma unroll
    for (int h = 0; h < 4; ++h) va[h] = VR_OFF + (4 * g + (c >> 2)) * 256 + ((4 * (h ^ (c >> 2)) + (c & 3)) << 4);

#define WAIT_FULL(need)                                                        \
  do {                                                                         \
    if (__builtin_amdgcn_readfirstlane(fpre) < (unsigned)(need)) {             \
      _Pragma("unroll 1") for (unsigned sp = 0; sp < SPIN_CAP; ++sp) {        \
        if (__builtin_amdgcn_readfirstlane(*FULL) >= (unsigned)(need)) break;  \
      }                                                                        \
    }                                                                          \
    MEMFENCE();                                                                \
  } while (0)
#define VREAD(so)                                                                                                    \
    _Pragma("unroll") for (int e = 0; e < 8; ++e) _Pragma("unroll") for (int u = 0; u < 2; ++u)                     \
        vf[e][u] = __builtin_bit_cast(u32x2, __builtin_amdgcn_ds_read_tr16_b64_v4i16(                                \
            (LAS s16x4*)(lds + (so) + va[e >> 1] + (e & 1) * 8 + u * 4096)));
#define PVMMA(P) _Pragma("unroll") for (int e = 0; e < 8; ++e) O[e] = mfma16(u32x4{vf[e][0][0], vf[e][0][1], vf[e][1][0], vf[e][1][1]}, P, O[e]);
#define STEP(j, P, FIRST)                                                                                            \
    {                                                                                                                \
      WAIT_FULL(max(16 * (j) - 12, 8 * (j) + 4));                                                                    \
      f32x4 S[2];                                \
      u32x4 kf[2][4];                                                                                                \
      const int so = ((j) & 7) * 8192;                                                                               \
      _Pragma("unroll") for (int s = 0; s < 4; ++s) _Pragma("unroll") for (int u = 0; u < 2; ++u)                   \
          kf[u][s] = *(const LAS u32x4*)(lds + so + ka[s] + u * 4096);                                               \
      SCHEDB();                                                                                                      \
      VREAD((max((j) - 2, 0) & 7) * 8192)                                                                            \
      SCHEDB();                                                                                                      \
      _Pragma("unroll") for (int s = 0; s < 4; ++s) _Pragma("unroll") for (int u = 0; u < 2; ++u)                   \
          S[u] = mfma16(kf[u][s], Q[s], s == 0 ? f32x4{0.f, 0.f, 0.f, 0.f} : S[u]);                                  \
      SCHEDB();                                                                                                      \
      PVMMA(P)                                                                                                       \
      asm volatile("s_waitcnt lgkmcnt(0)" ::: "memory");                        \
      DONE[v] = (j) + 1;                                                                                             \
      fpre = *FULL;                                                                                                  \
      if (FIRST && (j) == 0) {             \
        const float NB = -1000000000.0f / SCALE, ninf = -INFINITY;                                                   \
        const f32x4 bias = {(1.0f - __uint_as_float(mk[0] << 16)) * NB, (1.0f - __uint_as_float(mk[0] & 0xffff0000u)) * NB, \
                            (1.0f - __uint_as_float(mk[1] << 16)) * NB, (1.0f - __uint_as_float(mk[1] & 0xffff0000u)) * NB}; \
        S[0] = g < 2 ? S[0] + bias : f32x4{ninf, ninf, ninf, ninf};                                                  \
        S[1] = f32x4{ninf, ninf, ninf, ninf};                                                                        \
      }                                                                                                              \
      float x[8];                                                                     \
      _Pragma("unroll") for (int i = 0; i < 8; ++i) x[i] = S[i >> 2][i & 3] * CS;                                   \
      const float mx = xrow_max(max3(max3(x[0], x[1], x[2]), max3(x[3], x[4], x[5]), max3(x[6], x[7], m)));          \
      const float alpha = __builtin_amdgcn_exp2f(m - mx);                                                            \
      m = mx;                                                                                                        \
      _Pragma("unroll") for (int i = 0; i < 8; ++i) {                                                               \
        asm("" : "+v"(x[i]));                      \
        p[i] = __builtin_amdgcn_exp2f(x[i] - mx);                                                                    \
      }                                                                                                              \
      l = fmaf(l, alpha, ((p[0] + p[1]) + (p[2] + p[3])) + ((p[4] + p[5]) + (p[6] + p[7])));                         \
      P = u32x4{pack_bf16(p[0], p[1]), pack_bf16(p[2], p[3]), pack_bf16(p[4], p[5]), pack_bf16(p[6], p[7])};         \
      _Pragma("unroll") for (int e = 0; e < 8; ++e) O[e] *= alpha_p;        \
      alpha_p = alpha;                                                                                               \
    }

    unsigned fpre = 0;
    unsigned ta = 0, tb = 0;
    u32x4 pe[2] = {{0u, 0u, 0u, 0u}, {0u, 0u, 0u, 0u}};
    u32x2 vf[8][2];
#pragma unroll 1
    for (int j = 0;; j += 2) {
      STEP(j, PA, true)
      if (j == NTILE) break;
      if (v == 0) {
        if (j == JB + 2) {
          ta = FULL[2];
          tb = FULL[3];
          if (lane == 0) __hip_atomic_store(prog + blockIdx.x, ((unsigned long long)tb << 32) | ta, __ATOMIC_RELAXED, __HIP_MEMORY_SCOPE_AGENT);
        }
        if (j == JC) {
          const __amdgpu_buffer_rsrc_t prs = __builtin_amdgcn_make_buffer_rsrc((void*)prog, 0, NWG * 8, 0x00020000);
          pe[0] = __builtin_amdgcn_raw_buffer_load_b128(prs, lane * 32, 0, 16);
          pe[1] = __builtin_amdgcn_raw_buffer_load_b128(prs, lane * 32 + 16, 0, 16);
        }
        if (j == JC + 2) {
          float n = 0.f, sa = 0.f, sb = 0.f;
#pragma unroll
          for (int k = 0; k < 4; ++k) {
            const int da = (int)(pe[k >> 1][2 * (k & 1)] - ta), db = (int)(pe[k >> 1][2 * (k & 1) + 1] - tb);
            const bool ok = da > -300 && da < 300 && db > -300 && db < 300;
            n += ok ? 1.f : 0.f; sa += ok ? (float)da : 0.f; sb += ok ? (float)db : 0.f;
          }
          n = wave_sum(n); sa = wave_sum(sa); sb = wave_sum(sb);
          if (n >= 128.f) {
            const float rn = __builtin_amdgcn_rcpf(n);
            const float per = ((float)(int)(tb - ta) + (sb - sa) * rn) * (16.0f * PMARG / (JB - JA));
            const unsigned base = (tb << 4) + (unsigned)(int)(sb * rn * 16.0f - per * JB);
            if (lane == 0) *(volatile LAS u32x2*)SCHED = u32x2{base, (unsigned)per};
          }
        }
      }
      STEP(j + 1, PB, false)
    }
    const float inv = __builtin_amdgcn_rcpf(xrow_sum(l));
    PA = u32x4{pack_bf16(p[0] * inv, p[1] * inv), pack_bf16(p[2] * inv, p[3] * inv), pack_bf16(p[4] * inv, p[5] * inv), pack_bf16(p[6] * inv, p[7] * inv)};
    alpha_p *= inv;
    const __amdgpu_buffer_rsrc_t ors = __builtin_amdgcn_make_buffer_rsrc((void*)(out + ((size_t)bh * M_ + 16 * v) * D_), 0, 16 * D_ * 2, 0x00020000);
    asm volatile("" : "+v"(PA), "+v"(alpha_p));
    WAIT_FULL(NPIECE - 8);
    VREAD(((NTILE - 1) & 7) * 8192)
    fpre = *FULL;
    SCHEDB();
    PVMMA(PB)
    SCHEDB();
#pragma unroll
    for (int e = 0; e < 8; ++e) O[e] *= alpha_p;
    asm volatile("" : "+v"(O[0]), "+v"(O[1]), "+v"(O[2]), "+v"(O[3]), "+v"(O[4]), "+v"(O[5]), "+v"(O[6]), "+v"(O[7]));
    WAIT_FULL(NPIECE);
    VREAD((NTILE & 7) * 8192)
    SCHEDB();
    PVMMA(PA)
    SCHEDB();
#pragma unroll
    for (int e2 = 0; e2 < 4; ++e2) {
      const f32x4 a = O[2 * e2], b = O[2 * e2 + 1];
      __builtin_amdgcn_raw_buffer_store_b128(u32x4{pack_bf16(a[0], a[1]), pack_bf16(a[2], a[3]), pack_bf16(b[0], b[1]), pack_bf16(b[2], b[3])},
                                             ors, c * 256 + g * 16 + e2 * 64, 0, 2);
    }
  }
}
}

extern "C" void kernel_launch(void* const* d_in, const int* in_sizes, int n_in, void* d_out, int out_size, void* d_ws,
                              size_t ws_size, hipStream_t stream) {
  const u16* q = (const u16*)d_in[0];
  const u16* kc = (const u16*)d_in[1];
  const u16* vc = (const u16*)d_in[2];
  const u16* kt = (const u16*)d_in[3];
  const u16* vt = (const u16*)d_in[4];
  const u16* mask = (const u16*)d_in[5];
  (void)hipFuncSetAttribute((const void*)tree_attn_kernel, hipFuncAttributeMaxDynamicSharedMemorySize, LDS_BYTES);
  tree_attn_kernel<<<dim3(NWG), dim3(192), LDS_BYTES, stream>>>(q, kc, vc, kt, vt, mask, (u16*)d_out, (unsigned long long*)d_ws);
}
```

```cpp
#include <hip/hip_runtime.h>
#include <hip/hip_bf16.h>
#include <stdint.h>

#define WINL 20
#define WINB 28

namespace {
constexpr int B_ = 32, HQ = 32, HKV = 8, T_ = 8, C_ = 2048, D_ = 128;
constexpr int G_ = HQ / HKV;
constexpr int M_ = G_ * T_;
constexpr int NWG = B_ * HKV;
constexpr int NTILE = C_ / 32;
constexpr int NPIECE = 4 + NTILE * 16;
constexpr int JA = 4, JB = 18, JC = 24;
constexpr float PMARG = 1.003f;
static_assert(M_ == 32 && D_ == 128 && T_ == 8 && NWG == 256 && WINL >= 17 && WINL <= 24 && WINB <= 32, "layout assumptions");
static_assert(JB % 2 == 0 && JC % 2 == 0 && JC >= JB + 4, "exchange steps");

typedef __attribute__((ext_vector_type(8))) __bf16 bf16x8;
typedef __attribute__((ext_vector_type(4))) float f32x4;
typedef __attribute__((ext_vector_type(4))) unsigned u32x4;
typedef __attribute__((ext_vector_type(2))) unsigned u32x2;
typedef __attribute__((ext_vector_type(4))) short s16x4;
typedef unsigned short u16;
#define LAS __attribute__((address_space(3)))

constexpr float LOG2E = 1.4426950408889634f;
constexpr float SCALE = 0.08838834764831845f;
constexpr float CS = SCALE * LOG2E;

constexpr int VR_OFF = 65536, CTL_OFF = 131072, LDS_BYTES = CTL_OFF + 64;
constexpr unsigned SPIN_CAP = 1u << 18;

__device__ __forceinline__ f32x4 mfma16(u32x4 a, u32x4 b, f32x4 c) {
  return __builtin_amdgcn_mfma_f32_16x16x32_bf16(__builtin_bit_cast(bf16x8, a), __builtin_bit_cast(bf16x8, b), c, 0, 0, 0);
}
__device__ __forceinline__ unsigned pack_bf16(float lo, float hi) {
  typedef __attribute__((ext_vector_type(2))) float f32x2;
  typedef __attribute__((ext_vector_type(2))) __bf16 bf16x2;
  f32x2 v = {lo, hi};
  bf16x2 r = __builtin_convertvector(v, bf16x2);
  return __builtin_bit_cast(unsigned, r);
}
__device__ __forceinline__ float max3(float a, float b, float c) { return fmaxf(fmaxf(a, b), c); }
__device__ __forceinline__ float xrow_max(float x) {
  auto s = __builtin_amdgcn_permlane16_swap(__float_as_uint(x), __float_as_uint(x), false, false);
  x = fmaxf(__uint_as_float(s[0]), __uint_as_float(s[1]));
  auto t = __builtin_amdgcn_permlane32_swap(__float_as_uint(x), __float_as_uint(x), false, false);
  return fmaxf(__uint_as_float(t[0]), __uint_as_float(t[1]));
}
__device__ __forceinline__ float xrow_sum(float x) {
  auto s = __builtin_amdgcn_permlane16_swap(__float_as_uint(x), __float_as_uint(x), false, false);
  x = __uint_as_float(s[0]) + __uint_as_float(s[1]);
  auto t = __builtin_amdgcn_permlane32_swap(__float_as_uint(x), __float_as_uint(x), false, false);
  return __uint_as_float(t[0]) + __uint_as_float(t[1]);
}
__device__ __forceinline__ float wave_sum(float x) {
  x += __builtin_bit_cast(float, __builtin_amdgcn_mov_dpp(__builtin_bit_cast(int, x), 0xB1, 0xf, 0xf, true));
  x += __builtin_bit_cast(float, __builtin_amdgcn_mov_dpp(__builtin_bit_cast(int, x), 0x4E, 0xf, 0xf, true));
  x += __builtin_bit_cast(float, __builtin_amdgcn_mov_dpp(__builtin_bit_cast(int, x), 0x141, 0xf, 0xf, true));
  x += __builtin_bit_cast(float, __builtin_amdgcn_mov_dpp(__builtin_bit_cast(int, x), 0x140, 0xf, 0xf, true));
  return xrow_sum(x);
}
__device__ __forceinline__ u32x4 make_srd(const void* p, unsigned bytes) {
  const uint64_t a = (uint64_t)p;
  return u32x4{(unsigned)a, (unsigned)(a >> 32) & 0xffffu, bytes, 0x00020000u};
}
#define STR_(x) #x
#define STR(x) STR_(x)
#define SET_M0(x) asm volatile("s_mov_b32 m0, %0\n\ts_nop 0" :: "s"(x) : "memory")
#define DMAW(srd, voff, soff, IMM, W)                                                                            \
  asm volatile("buffer_load_dwordx4 %0, %1, %2 offen offset:" #IMM " nt lds\n\ts_waitcnt vmcnt(" STR(W) ")"      \
               :: "v"(voff), "s"(srd), "s"(soff) : "memory")
#define DMA(srd, voff, soff, IMM) DMAW(srd, voff, soff, IMM, WINL)
#define DMAB(srd, voff, soff, IMM) DMAW(srd, voff, soff, IMM, WINB)
#define DMA0(srd, voff, soff, IMM) DMAW(srd, voff, soff, IMM, 63)
#define MEMFENCE() asm volatile("" ::: "memory")
#define SCHEDB() __builtin_amdgcn_sched_barrier(0)

__global__ __launch_bounds__(192, 2) void tree_attn_kernel(const u16* q, const u16* kc, const u16* vc, const u16* kt, const u16* vt,
                                                            const u16* mask, u16* out, unsigned long long* prog) {
  extern __shared__ __attribute__((aligned(16))) char lds_generic[];
  LAS char* lds = (LAS char*)lds_generic;
  const int bh = (blockIdx.x & 7) * 32 + (blockIdx.x >> 3);
  const int lane = threadIdx.x & 63;
  const int wave = __builtin_amdgcn_readfirstlane(threadIdx.x >> 6);
  const int g = lane >> 4, c = lane & 15;
  volatile LAS unsigned* FULL = (volatile LAS unsigned*)(lds + CTL_OFF);
  volatile LAS unsigned* DONE = (volatile LAS unsigned*)(lds + CTL_OFF + 16);
  volatile LAS unsigned* SCHED = (volatile LAS unsigned*)(lds + CTL_OFF + 24);

    const unsigned lds0 = (unsigned)(uintptr_t)lds;
    const size_t base = (size_t)bh * C_ * D_;
    const u32x4 krs = make_srd(kc + base, C_ * D_ * 2), vrs = make_srd(vc + base, C_ * D_ * 2);
    const int kv0 = g * 256 + ((c ^ g) << 4), kv1 = kv0 ^ 64, kv2 = kv0 ^ 128, kv3 = kv0 ^ 192;
    const int vv0 = g * 256 + ((c ^ (g << 2)) << 4);
#define KBATCH(D, go, P, i)                                                                                          \
    SET_M0(lds0 + (((go) + 8192) & 65535));                                                                          \
    D(krs, kv0, go, 0); P(i + 1) D(krs, kv1, go, 1024); P(i + 2) D(krs, kv2, go, 2048); P(i + 3) D(krs, kv3, go, 3072); P(i + 4)
#define VBATCH(D, go, P, i)                                                                                          \
    SET_M0(lds0 + VR_OFF + (((go) + 8192) & 65535));                                                                 \
    D(vrs, vv0, go, 0); P(i + 1) D(vrs, vv0, go, 1024); P(i + 2) D(vrs, vv0, go, 2048); P(i + 3) D(vrs, vv0, go, 3072); P(i + 4)
#define NOPUB(i)
#define PUB(i) if ((i) == WINL - 16) *FULL = 4 + 16 * t; if ((i) == WINL - 8 && t == 0) *FULL = 12;
#define PUBB(i) if ((i) == WINB - 16) *FULL = 4 + 16 * t;
#define ROUND(D, so, P) KBATCH(D, so + 16384, P, 0) VBATCH(D, so, P, 4) KBATCH(D, so + 20480, P, 8) VBATCH(D, so + 4096, P, 12)
#define WAIT_DONE(need)                                                                                              \
    if (__builtin_expect((int)dmin < (need), 0)) {                                                                   \
      _Pragma("unroll 1") for (unsigned sp = 0; sp < SPIN_CAP; ++sp) {                                              \
        dmin = __builtin_amdgcn_readfirstlane(min(DONE[0], DONE[1]));                                                \
        if ((int)dmin >= (need)) break;                                                                              \
        __builtin_amdgcn_s_sleep(1);                                                                                 \
      }                                                                                                              \
      MEMFENCE();                                                                                                    \
    }
  if (wave == 0) {
    {
      const u32x4 ktrs = make_srd(kt + (size_t)bh * T_ * D_, T_ * D_ * 2), vtrs = make_srd(vt + (size_t)bh * T_ * D_, T_ * D_ * 2);
      SET_M0(lds0);
      DMA0(ktrs, kv0, 0, 0); DMA0(ktrs, kv1, 0, 1024);
      SET_M0(lds0 + VR_OFF);
      DMA0(vtrs, vv0, 0, 0); DMA0(vtrs, vv0, 0, 1024);
    }
    KBATCH(DMA0, 0, NOPUB, 0) KBATCH(DMA0, 4096, NOPUB, 0) KBATCH(DMA0, 8192, NOPUB, 0) KBATCH(DMA0, 12288, NOPUB, 0)
  }
  MEMFENCE();
  if (wave == 0) {
    __builtin_amdgcn_s_barrier();
    MEMFENCE();
    unsigned dmin = 0;
    unsigned sbase = 0, sper = 0;
#pragma unroll 1
    for (int t = 0; t < NTILE - 2; ++t) {
      WAIT_DONE(t - 4)
      const u32x4 cw = *(volatile LAS u32x4*)DONE;
      const int so = t * 8192;
      if (t == JA || t == JB) FULL[t == JA ? 2 : 3] = (unsigned)__builtin_amdgcn_s_memrealtime();
      int lag = 0;
      if (sper) {
        const unsigned due = sbase + t * sper;
        lag = (int)(((unsigned)__builtin_amdgcn_s_memrealtime() << 4) - due);
#pragma unroll 1
        for (int sp = 0; sp < 64 && lag < 0; ++sp) lag = (int)(((unsigned)__builtin_amdgcn_s_memrealtime() << 4) - due);
      }
      if (lag > 80) {
        ROUND(DMAB, so, PUBB)
      } else {
        ROUND(DMA, so, PUB)
      }
      dmin = __builtin_amdgcn_readfirstlane(min(cw[0], cw[1]));
      sbase = __builtin_amdgcn_readfirstlane(cw[2]);
      sper = __builtin_amdgcn_readfirstlane(cw[3]);
    }
    WAIT_DONE(NTILE - 5)
    {
      const int t = NTILE - 2;
      VBATCH(DMA, t * 8192, PUB, 0) VBATCH(DMA, t * 8192 + 4096, PUB, 4) VBATCH(DMA, t * 8192 + 8192, PUB, 8) VBATCH(DMA, t * 8192 + 12288, PUB, 12)
    }
#define DRAIN(n) asm volatile("s_waitcnt vmcnt(" #n ")" ::: "memory"); *FULL = NPIECE - n;
    DRAIN(16) DRAIN(8) DRAIN(0)
  } else {
    const int v = wave - 1;
    u32x4 Q[4];
    {
      const __amdgpu_buffer_rsrc_t qrs = __builtin_amdgcn_make_buffer_rsrc((void*)(q + ((size_t)bh * M_ + 16 * v) * D_), 0, 16 * D_ * 2, 0x00020000);
#pragma unroll
      for (int s = 0; s < 4; ++s) Q[s] = __builtin_amdgcn_raw_buffer_load_b128(qrs, c * 256 + g * 16 + s * 64, 0, 2);
    }
    const __amdgpu_buffer_rsrc_t mrs = __builtin_amdgcn_make_buffer_rsrc((void*)(mask + (size_t)(bh / HKV) * T_ * T_), 0, T_ * T_ * 2, 0x00020000);
    u32x2 mk = __builtin_amdgcn_raw_buffer_load_b64(mrs, (c & 7) * 16 + (g & 1) * 8, 0, 2);
#pragma unroll
    for (int j = 0; j < 3; ++j) *(LAS u32x4*)(lds + VR_OFF + 2048 + (v * 3 + j) * 1024 + lane * 16) = u32x4{0u, 0u, 0u, 0u};
    if (v == 0 && lane < 8) FULL[lane] = 0u;
    asm volatile("s_waitcnt lgkmcnt(0)" ::: "memory");
    __builtin_amdgcn_s_barrier();
    asm volatile("" : "+v"(mk) :: "memory");

    float m = -INFINITY, l = 0.f;
    f32x4 O[8];
#pragma unroll
    for (int e = 0; e < 8; ++e) O[e] = f32x4{0.f, 0.f, 0.f, 0.f};
    u32x4 PA = {0u, 0u, 0u, 0u}, PB = {0u, 0u, 0u, 0u};
    float alpha_p = 1.f;
    float pa[8], pb[8];
    int ka[4], va[4];
#pragma unroll
    for (int s = 0; s < 4; ++s) ka[s] = c * 256 + (((4 * s + g) ^ c) << 4);
#pragma unroll
    for (int h = 0; h < 4; ++h) va[h] = VR_OFF + (4 * g + (c >> 2)) * 256 + ((4 * (h ^ (c >> 2)) + (c & 3)) << 4);

#define WAIT_FULL(need)                                                        \
  do {                                                                         \
    if (__builtin_amdgcn_readfirstlane(fpre) < (unsigned)(need)) {             \
      _Pragma("unroll 1") for (unsigned sp = 0; sp < SPIN_CAP; ++sp) {        \
        if (__builtin_amdgcn_readfirstlane(*FULL) >= (unsigned)(need)) break;  \
      }                                                                        \
    }                                                                          \
    MEMFENCE();                                                                \
  } while (0)
#define VREAD(so) VREADX(vf, so)
#define VREADX(vf, so)                                                                                               \
    _Pragma("unroll") for (int e = 0; e < 8; ++e) _Pragma("unroll") for (int u = 0; u < 2; ++u)                     \
        vf[e][u] = __builtin_bit_cast(u32x2, __builtin_amdgcn_ds_read_tr16_b64_v4i16(                                \
            (LAS s16x4*)(lds + (so) + va[e >> 1] + (e & 1) * 8 + u * 4096)));
#define PVMMA(P) PVMMAX(vf, P)
#define PVMMAX(vf, P) _Pragma("unroll") for (int e = 0; e < 8; ++e) O[e] = mfma16(u32x4{vf[e][0][0], vf[e][0][1], vf[e][1][0], vf[e][1][1]}, P, O[e]);
#define STEP(j, P, p, FIRST)                                                                                           \
    {                                                                                                                \
      WAIT_FULL(max(16 * (j) - 12, 8 * (j) + 4));                                                                    \
      f32x4 S[2];                                \
      u32x4 kf[2][4];                                                                                                \
      const int so = ((j) & 7) * 8192;                                                                               \
      _Pragma("unroll") for (int s = 0; s < 4; ++s) _Pragma("unroll") for (int u = 0; u < 2; ++u)                   \
          kf[u][s] = *(const LAS u32x4*)(lds + so + ka[s] + u * 4096);                                               \
      SCHEDB();                                                                                                      \
      VREAD((max((j) - 2, 0) & 7) * 8192)                                                                            \
      SCHEDB();                                                                                                      \
      _Pragma("unroll") for (int s = 0; s < 4; ++s) _Pragma("unroll") for (int u = 0; u < 2; ++u)                   \
          S[u] = mfma16(kf[u][s], Q[s], s == 0 ? f32x4{0.f, 0.f, 0.f, 0.f} : S[u]);                                  \
      SCHEDB();                                                                                                      \
      PVMMA(P)                                                                                                       \
      asm volatile("s_waitcnt lgkmcnt(0)" ::: "memory");                        \
      DONE[v] = (j) + 1;                                                                                             \
      fpre = *FULL;                                                                                                  \
      if (FIRST && (j) == 0) {             \
        const float NB = -1000000000.0f / SCALE, ninf = -INFINITY;                                                   \
        const f32x4 bias = {(1.0f - __uint_as_float(mk[0] << 16)) * NB, (1.0f - __uint_as_float(mk[0] & 0xffff0000u)) * NB, \
                            (1.0f - __uint_as_float(mk[1] << 16)) * NB, (1.0f - __uint_as_float(mk[1] & 0xffff0000u)) * NB}; \
        S[0] = g < 2 ? S[0] + bias : f32x4{ninf, ninf, ninf, ninf};                                                  \
        S[1] = f32x4{ninf, ninf, ninf, ninf};                                                                        \
      }                                                                                                              \
      float x[8];                                                                     \
      _Pragma("unroll") for (int i = 0; i < 8; ++i) x[i] = S[i >> 2][i & 3] * CS;                                   \
      const float mx = xrow_max(max3(max3(x[0], x[1], x[2]), max3(x[3], x[4], x[5]), max3(x[6], x[7], m)));          \
      const float alpha = __builtin_amdgcn_exp2f(m - mx);                                                            \
      m = mx;                                                                                                        \
      _Pragma("unroll") for (int i = 0; i < 8; ++i) {                                                               \
        asm("" : "+v"(x[i]));                      \
        p[i] = __builtin_amdgcn_exp2f(x[i] - mx);                                                                    \
      }                                                                                                              \
      l = fmaf(l, alpha, ((p[0] + p[1]) + (p[2] + p[3])) + ((p[4] + p[5]) + (p[6] + p[7])));                         \
      P = u32x4{pack_bf16(p[0], p[1]), pack_bf16(p[2], p[3]), pack_bf16(p[4], p[5]), pack_bf16(p[6], p[7])};         \
      _Pragma("unroll") for (int k_ = 0; k_ < 8; ++k_) {             \
        __builtin_amdgcn_sched_group_barrier(0x8, 1, 0);                                                             \
        __builtin_amdgcn_sched_group_barrier(0x2, 3, 0);                                                             \
      }                                                                                                              \
      if (__builtin_amdgcn_ballot_w64(alpha_p != 1.0f) != 0) {     \
        _Pragma("unroll") for (int e = 0; e < 8; ++e) O[e] *= alpha_p;                                               \
      }                                                                                                              \
      alpha_p = alpha;                                                                                               \
    }

    unsigned fpre = 0;
    unsigned ta = 0, tb = 0;
    u32x4 pe[2] = {{0u, 0u, 0u, 0u}, {0u, 0u, 0u, 0u}};
    u32x2 vf[8][2];
#pragma unroll 1
    for (int j = 0;; j += 2) {
      STEP(j, PA, pa, true)
      if (j == NTILE) break;
      if (v == 0) {
        if (j == JB + 2) {
          ta = FULL[2];
          tb = FULL[3];
          if (lane == 0) __hip_atomic_store(prog + blockIdx.x, ((unsigned long long)tb << 32) | ta, __ATOMIC_RELAXED, __HIP_MEMORY_SCOPE_AGENT);
        }
        if (j == JC) {
          const __amdgpu_buffer_rsrc_t prs = __builtin_amdgcn_make_buffer_rsrc((void*)prog, 0, NWG * 8, 0x00020000);
          pe[0] = __builtin_amdgcn_raw_buffer_load_b128(prs, lane * 32, 0, 16);
          pe[1] = __builtin_amdgcn_raw_buffer_load_b128(prs, lane * 32 + 16, 0, 16);
        }
        if (j == JC + 2) {
          float n = 0.f, sa = 0.f, sb = 0.f;
#pragma unroll
          for (int k = 0; k < 4; ++k) {
            const int da = (int)(pe[k >> 1][2 * (k & 1)] - ta), db = (int)(pe[k >> 1][2 * (k & 1) + 1] - tb);
            const bool ok = da > -300 && da < 300 && db > -300 && db < 300;
            n += ok ? 1.f : 0.f; sa += ok ? (float)da : 0.f; sb += ok ? (float)db : 0.f;
          }
          n = wave_sum(n); sa = wave_sum(sa); sb = wave_sum(sb);
          if (n >= 128.f) {
            const float rn = __builtin_amdgcn_rcpf(n);
            const float per = ((float)(int)(tb - ta) + (sb - sa) * rn) * (16.0f * PMARG / (JB - JA));
            const unsigned base = (tb << 4) + (unsigned)(int)(sb * rn * 16.0f - per * JB);
            if (lane == 0) *(volatile LAS u32x2*)SCHED = u32x2{base, (unsigned)per};
          }
        }
      }
      STEP(j + 1, PB, pb, false)
    }
    const float inv = __builtin_amdgcn_rcpf(xrow_sum(l));
    const float fl = alpha_p * inv;
    PA = u32x4{pack_bf16(pa[0] * inv, pa[1] * inv), pack_bf16(pa[2] * inv, pa[3] * inv), pack_bf16(pa[4] * inv, pa[5] * inv), pack_bf16(pa[6] * inv, pa[7] * inv)};
    PB = u32x4{pack_bf16(pb[0] * fl, pb[1] * fl), pack_bf16(pb[2] * fl, pb[3] * fl), pack_bf16(pb[4] * fl, pb[5] * fl), pack_bf16(pb[6] * fl, pb[7] * fl)};
    const __amdgpu_buffer_rsrc_t ors = __builtin_amdgcn_make_buffer_rsrc((void*)(out + ((size_t)bh * M_ + 16 * v) * D_), 0, 16 * D_ * 2, 0x00020000);
    WAIT_FULL(NPIECE - 8);
    VREAD(((NTILE - 1) & 7) * 8192)
    fpre = *FULL;
    SCHEDB();
#pragma unroll
    for (int e = 0; e < 8; ++e) O[e] *= fl;
    asm volatile("" : "+v"(PA), "+v"(PB), "+v"(O[0]), "+v"(O[1]), "+v"(O[2]), "+v"(O[3]), "+v"(O[4]), "+v"(O[5]), "+v"(O[6]), "+v"(O[7]));
    WAIT_FULL(NPIECE);
    u32x2 vg[8][2];
    VREADX(vg, (NTILE & 7) * 8192)
    SCHEDB();
    PVMMA(PB)
    PVMMAX(vg, PA)
    SCHEDB();
#pragma unroll
    for (int e2 = 0; e2 < 4; ++e2) {
      const f32x4 a = O[2 * e2], b = O[2 * e2 + 1];
      __builtin_amdgcn_raw_buffer_store_b128(u32x4{pack_bf16(a[0], a[1]), pack_bf16(a[2], a[3]), pack_bf16(b[0], b[1]), pack_bf16(b[2], b[3])},
                                             ors, c * 256 + g * 16 + e2 * 64, 0, 2);
    }
  }
}
}

extern "C" void kernel_launch(void* const* d_in, const int* in_sizes, int n_in, void* d_out, int out_size, void* d_ws,
                              size_t ws_size, hipStream_t stream) {
  const u16* q = (const u16*)d_in[0];
  const u16* kc = (const u16*)d_in[1];
  const u16* vc = (const u16*)d_in[2];
  const u16* kt = (const u16*)d_in[3];
  const u16* vt = (const u16*)d_in[4];
  const u16* mask = (const u16*)d_in[5];
  (void)hipFuncSetAttribute((const void*)tree_attn_kernel, hipFuncAttributeMaxDynamicSharedMemorySize, LDS_BYTES);
  tree_attn_kernel<<<dim3(NWG), dim3(192), LDS_BYTES, stream>>>(q, kc, vc, kt, vt, mask, (u16*)d_out, (unsigned long long*)d_ws);
}
```

```cpp
#include <hip/hip_runtime.h>
#include <hip/hip_bf16.h>
#include <stdint.h>

#define WINL 20
#define WINB 28
#define JAV 6
#define JBV 14
#define PMARGV 1.003f
#define CODEPF 0
#define KILV 3
#define STAMPS 0

namespace {
constexpr int B_ = 32, HQ = 32, HKV = 8, T_ = 8, C_ = 2048, D_ = 128;
constexpr int G_ = HQ / HKV;
constexpr int M_ = G_ * T_;
constexpr int NWG = B_ * HKV;
constexpr int PH = 16;
constexpr int NROUND = 4 * PH;
constexpr int NPIECE = 4 + NROUND * 16;
constexpr int JA = JAV, JB = JBV;
constexpr float PMARG = PMARGV;
static_assert(M_ == 32 && D_ == 128 && T_ == 8 && NWG == 256 && C_ == 2 * PH * 64 && WINL >= 17 && WINL <= 24 && WINB <= 32 && JB % 2 == 0, "layout");

typedef __attribute__((ext_vector_type(8))) __bf16 bf16x8;
typedef __attribute__((ext_vector_type(4))) float f32x4;
typedef __attribute__((ext_vector_type(4))) unsigned u32x4;
typedef __attribute__((ext_vector_type(2))) unsigned u32x2;
typedef __attribute__((ext_vector_type(4))) short s16x4;
typedef unsigned short u16;
#define LAS __attribute__((address_space(3)))

constexpr float LOG2E = 1.4426950408889634f;
constexpr float SCALE = 0.08838834764831845f;
constexpr float CS = SCALE * LOG2E;

constexpr int RING = 5 * 16384, TREE_OFF = 4 * 16384, PS_OFF = RING, AS_OFF = PS_OFF + PH * 4096, CTL_OFF = AS_OFF + PH * 512;
constexpr int TS_OFF = CTL_OFF + 64;
constexpr int LDS_BYTES = CTL_OFF + 64 + (STAMPS ? 3 * 40 * 8 : 0);
constexpr unsigned SPIN_CAP = 1u << 18;

__device__ __forceinline__ f32x4 mfma16(u32x4 a, u32x4 b, f32x4 c) {
  return __builtin_amdgcn_mfma_f32_16x16x32_bf16(__builtin_bit_cast(bf16x8, a), __builtin_bit_cast(bf16x8, b), c, 0, 0, 0);
}
__device__ __forceinline__ unsigned pack_bf16(float lo, float hi) {
  typedef __attribute__((ext_vector_type(2))) float f32x2;
  typedef __attribute__((ext_vector_type(2))) __bf16 bf16x2;
  f32x2 v = {lo, hi};
  bf16x2 r = __builtin_convertvector(v, bf16x2);
  return __builtin_bit_cast(unsigned, r);
}
__device__ __forceinline__ float max3(float a, float b, float c) { return fmaxf(fmaxf(a, b), c); }
__device__ __forceinline__ float exp2c(float x) { return __builtin_amdgcn_fmed3f(__builtin_amdgcn_exp2f(x), 0.f, 1.f); }
__device__ __forceinline__ float xrow_max(float x) {
  auto s = __builtin_amdgcn_permlane16_swap(__float_as_uint(x), __float_as_uint(x), false, false);
  x = fmaxf(__uint_as_float(s[0]), __uint_as_float(s[1]));
  auto t = __builtin_amdgcn_permlane32_swap(__float_as_uint(x), __float_as_uint(x), false, false);
  return fmaxf(__uint_as_float(t[0]), __uint_as_float(t[1]));
}
__device__ __forceinline__ float xrow_sum(float x) {
  auto s = __builtin_amdgcn_permlane16_swap(__float_as_uint(x), __float_as_uint(x), false, false);
  x = __uint_as_float(s[0]) + __uint_as_float(s[1]);
  auto t = __builtin_amdgcn_permlane32_swap(__float_as_uint(x), __float_as_uint(x), false, false);
  return __uint_as_float(t[0]) + __uint_as_float(t[1]);
}
__device__ __forceinline__ float wave_sum(float x) {
  x += __builtin_bit_cast(float, __builtin_amdgcn_mov_dpp(__builtin_bit_cast(int, x), 0xB1, 0xf, 0xf, true));
  x += __builtin_bit_cast(float, __builtin_amdgcn_mov_dpp(__builtin_bit_cast(int, x), 0x4E, 0xf, 0xf, true));
  x += __builtin_bit_cast(float, __builtin_amdgcn_mov_dpp(__builtin_bit_cast(int, x), 0x141, 0xf, 0xf, true));
  x += __builtin_bit_cast(float, __builtin_amdgcn_mov_dpp(__builtin_bit_cast(int, x), 0x140, 0xf, 0xf, true));
  return xrow_sum(x);
}
__device__ __forceinline__ u32x4 make_srd(const void* p, unsigned bytes) {
  const uint64_t a = (uint64_t)p;
  return u32x4{(unsigned)a, (unsigned)(a >> 32) & 0xffffu, bytes, 0x00020000u};
}
#define STR_(x) #x
#define STR(x) STR_(x)
#define SET_M0(x) asm volatile("s_mov_b32 m0, %0\n\ts_nop 0" :: "s"(x) : "memory")
#define DMAW(srd, voff, soff, IMM, W)                                                                            \
  asm volatile("buffer_load_dwordx4 %0, %1, %2 offen offset:" #IMM " nt lds\n\ts_waitcnt vmcnt(" STR(W) ")"      \
               :: "v"(voff), "s"(srd), "s"(soff) : "memory")
#define DMA(srd, voff, soff, IMM) DMAW(srd, voff, soff, IMM, WINL)
#define DMAB(srd, voff, soff, IMM) DMAW(srd, voff, soff, IMM, WINB)
#define DMA0(srd, voff, soff, IMM) DMAW(srd, voff, soff, IMM, 63)
#define MEMFENCE() asm volatile("" ::: "memory")
#define LGKM0() do { MEMFENCE(); __builtin_amdgcn_s_waitcnt(0xc07f); MEMFENCE(); } while (0)
#define SCHEDB() __builtin_amdgcn_sched_barrier(0)

__global__ __launch_bounds__(192, 2) void tree_attn_kernel(const u16* q, const u16* kc, const u16* vc, const u16* kt, const u16* vt,
                                                            const u16* mask, u16* out, unsigned long long* prog
) {
  extern __shared__ __attribute__((aligned(16))) char lds_generic[];
  LAS char* lds = (LAS char*)lds_generic;
#define TSTAMP2()
  const int bh = (blockIdx.x & 7) * 32 + (blockIdx.x >> 3);
  const int lane = threadIdx.x & 63;
  const int wave = __builtin_amdgcn_readfirstlane(threadIdx.x >> 6);
  const int g = lane >> 4, c = lane & 15;
  volatile LAS unsigned* FULL = (volatile LAS unsigned*)(lds + CTL_OFF);
  volatile LAS unsigned* DONE = (volatile LAS unsigned*)(lds + CTL_OFF + 16);
  volatile LAS unsigned* SCHED = (volatile LAS unsigned*)(lds + CTL_OFF + 24);

  const unsigned lds0 = (unsigned)(uintptr_t)lds;
  const size_t base = (size_t)bh * C_ * D_;
  const int kv0 = g * 256 + ((c ^ g) << 4), vv0 = g * 256 + ((c ^ (g << 2)) << 4);
  u32x4 srd = make_srd(kc + base, C_ * D_ * 2);
  int o0 = kv0, o1 = kv0 ^ 64, o2 = kv0 ^ 128, o3 = kv0 ^ 192;
#define BATCH(D, go, ro, P, i)                                                                                       \
    SET_M0(lds0 + (ro));                                                                                             \
    D(srd, o0, go, 0); P(i + 1) D(srd, o1, go, 1024); P(i + 2) D(srd, o2, go, 2048); P(i + 3) D(srd, o3, go, 3072); P(i + 4)
#define ROUND(D, go, ro, P) BATCH(D, go, ro, P, 0) BATCH(D, go + 4096, ro + 4096, P, 4) BATCH(D, go + 8192, ro + 8192, P, 8) BATCH(D, go + 12288, ro + 12288, P, 12)
#define NOPUB(i)
#define PUB(i) if ((i) == WINL - 16) *FULL = 16 * r - 12;
#define PUBB(i) if ((i) == WINB - 16) *FULL = 16 * r - 12;
  if (wave == 0) {
    const u32x4 ktrs = make_srd(kt + (size_t)bh * T_ * D_, T_ * D_ * 2), vtrs = make_srd(vt + (size_t)bh * T_ * D_, T_ * D_ * 2);
    SET_M0(lds0 + TREE_OFF);
    DMA0(ktrs, o0, 0, 0); DMA0(ktrs, o1, 0, 1024);
    SET_M0(lds0 + TREE_OFF + 8192);
    DMA0(vtrs, vv0, 0, 0); DMA0(vtrs, vv0, 0, 1024);
    ROUND(DMA0, 0, 0, NOPUB)
  }
  MEMFENCE();
  if (wave == 0) {
    __builtin_amdgcn_s_barrier();
    MEMFENCE();
    unsigned dmin = 0;
    unsigned sbase = 0, sper = 0;
    int ro = 16384;
#pragma unroll 1
    for (int r = 1; r < NROUND; ++r) {
      if ((r & (PH - 1)) == 0) {
        const bool isv = (r / PH) & 1;
        srd = make_srd((isv ? vc : kc) + base, C_ * D_ * 2);
        o0 = isv ? vv0 : kv0; o1 = isv ? vv0 : kv0 ^ 64; o2 = isv ? vv0 : kv0 ^ 128; o3 = isv ? vv0 : kv0 ^ 192;
      }
      if (__builtin_expect((int)dmin < r - 3, 0)) {
#pragma unroll 1
        for (unsigned sp = 0; sp < SPIN_CAP; ++sp) {
          dmin = __builtin_amdgcn_readfirstlane(min(DONE[0], DONE[1]));
          if ((int)dmin >= r - 3) break;
        }
        MEMFENCE();
      }
      const int go = ((r / (2 * PH)) * PH + (r & (PH - 1))) * 16384;
      if (r == JA || r == JB) FULL[r == JA ? 2 : 3] = (unsigned)__builtin_amdgcn_s_memrealtime();
      int lag = 0;
      if (sper) {
        const unsigned due = sbase + r * sper;
        lag = (int)(((unsigned)__builtin_amdgcn_s_memrealtime() << 4) - due);
#pragma unroll 1
        for (int sp = 0; sp < 64 && lag < 0; ++sp) lag = (int)(((unsigned)__builtin_amdgcn_s_memrealtime() << 4) - due);
      }
      u32x4 cw;
      if (lag > 80) {
        BATCH(DMAB, go, ro, PUBB, 0) BATCH(DMAB, go + 4096, ro + 4096, PUBB, 4) BATCH(DMAB, go + 8192, ro + 8192, PUBB, 8)
        cw = *(volatile LAS u32x4*)DONE;
        BATCH(DMAB, go + 12288, ro + 12288, PUBB, 12)
      } else {
        BATCH(DMA, go, ro, PUB, 0) BATCH(DMA, go + 4096, ro + 4096, PUB, 4) BATCH(DMA, go + 8192, ro + 8192, PUB, 8)
        cw = *(volatile LAS u32x4*)DONE;
        BATCH(DMA, go + 12288, ro + 12288, PUB, 12)
      }
      ro = ro == RING - 16384 ? 0 : ro + 16384;
      dmin = __builtin_amdgcn_readfirstlane(min(cw[0], cw[1]));
      sbase = __builtin_amdgcn_readfirstlane(cw[2]);
      sper = __builtin_amdgcn_readfirstlane(cw[3]);
      if ((r & 1) == 1) TSTAMP2();
    }
#define DRAIN(n) asm volatile("s_waitcnt vmcnt(" #n ")" ::: "memory"); *FULL = NPIECE - n;
    DRAIN(16) DRAIN(8) DRAIN(0)
  } else {
    const int v = wave - 1;
    u32x4 Q[4];
    {
      const __amdgpu_buffer_rsrc_t qrs = __builtin_amdgcn_make_buffer_rsrc((void*)(q + ((size_t)bh * M_ + 16 * v) * D_), 0, 16 * D_ * 2, 0x00020000);
#pragma unroll
      for (int s = 0; s < 4; ++s) Q[s] = __builtin_amdgcn_raw_buffer_load_b128(qrs, c * 256 + g * 16 + s * 64, 0, 2);
    }
    const __amdgpu_buffer_rsrc_t mrs = __builtin_amdgcn_make_buffer_rsrc((void*)(mask + (size_t)(bh / HKV) * T_ * T_), 0, T_ * T_ * 2, 0x00020000);
    u32x2 mk = __builtin_amdgcn_raw_buffer_load_b64(mrs, (c & 7) * 16 + (g & 1) * 8, 0, 2);
    *(LAS u32x4*)(lds + TREE_OFF + 8192 + 2048 + v * 1024 + lane * 16) = u32x4{0u, 0u, 0u, 0u};
    if (v == 0 && lane < 8) FULL[lane] = 0u;
    LGKM0();
    __builtin_amdgcn_s_barrier();
    asm volatile("" : "+v"(mk) :: "memory");

    int ka[4], va[4];
#pragma unroll
    for (int s = 0; s < 4; ++s) ka[s] = c * 256 + (((4 * s + g) ^ c) << 4);
#pragma unroll
    for (int h = 0; h < 4; ++h) va[h] = (4 * g + (c >> 2)) * 256 + ((4 * (h ^ (c >> 2)) + (c & 3)) << 4);
    LAS char* pst = lds + PS_OFF + v * 2048 + lane * 16;
    LAS char* ast = lds + AS_OFF + v * 256 + lane * 4;

#define WAIT_FULL(need)                                                        \
  do {                                                                         \
    if (__builtin_amdgcn_readfirstlane(fpre) < (unsigned)(need)) {             \
      _Pragma("unroll 1") for (unsigned sp = 0; sp < SPIN_CAP; ++sp) {        \
        if (__builtin_amdgcn_readfirstlane(*FULL) >= (unsigned)(need)) break;  \
      }                                                                        \
    }                                                                          \
    MEMFENCE();                                                                \
  } while (0)
#define TRREAD(off) __builtin_bit_cast(u32x2, __builtin_amdgcn_ds_read_tr16_b64_v4i16((LAS s16x4*)(lds + (off))))
#define VREAD(vf, so)                                                                                                \
    _Pragma("unroll") for (int e = 0; e < 8; ++e) _Pragma("unroll") for (int u = 0; u < 2; ++u)                     \
        vf[e][u] = TRREAD((so) + va[e >> 1] + (e & 1) * 8 + u * 4096);
#define PVMMA(vf, P) _Pragma("unroll") for (int e = 0; e < 8; ++e) O[e] = mfma16(u32x4{vf[e][0][0], vf[e][0][1], vf[e][1][0], vf[e][1][1]}, P, O[e]);

    unsigned fpre = 0;
    float m;
    f32x4 L;
    const u32x4 ONES = {0x3f803f80u, 0x3f803f80u, 0x3f803f80u, 0x3f803f80u};
    f32x4 O[8];
    WAIT_FULL(4);
    {
      f32x4 S = {0.f, 0.f, 0.f, 0.f};
#pragma unroll
      for (int s = 0; s < 4; ++s) S = mfma16(*(const LAS u32x4*)(lds + TREE_OFF + ka[s]), Q[s], S);
      const float NB = -1000000000.0f * LOG2E;
      const float mv[4] = {__uint_as_float(mk[0] << 16), __uint_as_float(mk[0] & 0xffff0000u), __uint_as_float(mk[1] << 16), __uint_as_float(mk[1] & 0xffff0000u)};
      float x[4], p[4];
#pragma unroll
      for (int i = 0; i < 4; ++i) x[i] = g < 2 ? fmaf(S[i], CS, (1.0f - mv[i]) * NB) : -INFINITY;
      m = xrow_max(fmaxf(fmaxf(x[0], x[1]), fmaxf(x[2], x[3])));
#pragma unroll
      for (int i = 0; i < 4; ++i) p[i] = __builtin_amdgcn_exp2f(x[i] - m);
      const u32x4 P = {pack_bf16(p[0], p[1]), pack_bf16(p[2], p[3]), 0u, 0u};
      L = mfma16(ONES, P, f32x4{0.f, 0.f, 0.f, 0.f});
#pragma unroll
      for (int e = 0; e < 8; ++e) {
        const u32x2 t = TRREAD(TREE_OFF + 8192 + va[e >> 1] + (e & 1) * 8);
        O[e] = mfma16(u32x4{t[0], t[1], 0u, 0u}, P, f32x4{0.f, 0.f, 0.f, 0.f});
      }
      LGKM0();
      DONE[v] = 1;
    }

#define EXCHANGE() \
        if (v == 0 && r >= JB && r <= JB + 6) { \
          if (r == JB) { \
            ta = FULL[2]; \
            tb = FULL[3]; \
            if (lane == 0) __hip_atomic_store(prog + blockIdx.x, ((unsigned long long)tb << 32) | ta, __ATOMIC_RELAXED, __HIP_MEMORY_SCOPE_AGENT); \
          } \
          if (r == JB + 4) { \
            const __amdgpu_buffer_rsrc_t prs = __builtin_amdgcn_make_buffer_rsrc((void*)prog, 0, NWG * 8, 0x00020000); \
            pe[0] = __builtin_amdgcn_raw_buffer_load_b128(prs, lane * 32, 0, 16); \
            pe[1] = __builtin_amdgcn_raw_buffer_load_b128(prs, lane * 32 + 16, 0, 16); \
          } \
          if (r == JB + 6) { \
            float n = 0.f, sa = 0.f, sb = 0.f; \
          _Pragma("unroll") \
            for (int j = 0; j < 4; ++j) { \
              const int da = (int)(pe[j >> 1][2 * (j & 1)] - ta), db = (int)(pe[j >> 1][2 * (j & 1) + 1] - tb); \
              const bool ok = da > -150 && da < 150 && db > -150 && db < 150; \
              n += ok ? 1.f : 0.f; sa += ok ? (float)da : 0.f; sb += ok ? (float)db : 0.f; \
            } \
            n = wave_sum(n); sa = wave_sum(sa); sb = wave_sum(sb); \
            if (n >= 128.f) { \
              const float rn = __builtin_amdgcn_rcpf(n); \
              const float per = ((float)(int)(tb - ta) + (sb - sa) * rn) * (16.0f * PMARG / (JB - JA)); \
              const unsigned sb0 = (tb << 4) + (unsigned)(int)(sb * rn * 16.0f - per * JB); \
              if (lane == 0) *(volatile LAS u32x2*)SCHED = u32x2{sb0, (unsigned)per}; \
            } \
          } \
        }
    unsigned ta = 0, tb = 0;
    u32x4 pe[2] = {{0u, 0u, 0u, 0u}, {0u, 0u, 0u, 0u}};
    int ro = 0, r = 0;
    u32x2 vf[8][2], vg[8][2];
    u32x4 P0, P1;
    float alpha;
#pragma unroll 1
    for (int half = 0; half < 2; ++half) {
#define KREAD()                                                                                                      \
      _Pragma("unroll") for (int s = 0; s < 4; ++s) _Pragma("unroll") for (int u = 0; u < 4; ++u)                   \
          kf[u][s] = *(const LAS u32x4*)(lds + ro + ka[s] + u * 4096);
#define KMMA(S)                                  \
      _Pragma("unroll") for (int s = 0; s < 4; ++s) _Pragma("unroll") for (int u = 0; u < 4; ++u)                   \
          S[u] = mfma16(kf[u][s], Q[s], s == 0 ? f32x4{0.f, 0.f, 0.f, 0.f} : S[u]);
#define KSTEP(S, SN)                                                                                                 \
      {                                                                                                              \
        const bool more = k + 1 < PH;     \
        WAIT_FULL(more ? 16 * r + 36 : 0);                                                                           \
        KREAD()                                                                                                      \
        fpre = *FULL;                                                                                                \
        SCHEDB();                                                                                                    \
        const float mr = max3(max3(max3(S[0][0], S[0][1], S[0][2]), max3(S[0][3], S[1][0], S[1][1]), max3(S[1][2], S[1][3], S[2][0])), \
                              max3(S[2][1], S[2][2], S[2][3]), max3(max3(S[3][0], S[3][1], S[3][2]), S[3][3], S[3][3])); \
        const float mx = fmaxf(m, xrow_max(mr) * CS);                                    \
        alpha = __builtin_amdgcn_exp2f(m - mx);                                                                      \
        m = mx;                                                                                                      \
        L *= alpha;                                                                                                  \
        LGKM0();                                                                       \
        DONE[v] = more ? r + 3 : r + 2;                                                                              \
        SCHEDB();                                                                                                    \
        KMMA(SN)                                                                                                     \
        float p[16];                                                                                                 \
        _Pragma("unroll") for (int i = 0; i < 16; ++i) p[i] = exp2c(fmaf(S[i >> 2][i & 3], CS, -mx));               \
        P0 = u32x4{pack_bf16(p[0], p[1]), pack_bf16(p[2], p[3]), pack_bf16(p[4], p[5]), pack_bf16(p[6], p[7])};      \
        P1 = u32x4{pack_bf16(p[8], p[9]), pack_bf16(p[10], p[11]), pack_bf16(p[12], p[13]), pack_bf16(p[14], p[15])}; \
        _Pragma("unroll") for (int k_ = 0; k_ < 16; ++k_) {                                                         \
          __builtin_amdgcn_sched_group_barrier(0x8, 1, 0);                                                           \
          __builtin_amdgcn_sched_group_barrier(0x402, KILV, 0);                                                      \
        }                                                                                                            \
        SCHEDB();                                                                                                    \
        L = mfma16(ONES, P0, L);      \
        L = mfma16(ONES, P1, L);                                                                                     \
        if (r == 3 * PH - 1) {                    \
          const float inv = __builtin_amdgcn_rcpf(L[0]);                                                             \
          alpha *= inv;                                                                                              \
          P0 = u32x4{pack_bf16(p[0] * inv, p[1] * inv), pack_bf16(p[2] * inv, p[3] * inv), pack_bf16(p[4] * inv, p[5] * inv), pack_bf16(p[6] * inv, p[7] * inv)}; \
          P1 = u32x4{pack_bf16(p[8] * inv, p[9] * inv), pack_bf16(p[10] * inv, p[11] * inv), pack_bf16(p[12] * inv, p[13] * inv), pack_bf16(p[14] * inv, p[15] * inv)}; \
        }                                                                                                            \
        *(LAS u32x4*)(pst + k * 4096) = P0;                                                                          \
        *(LAS u32x4*)(pst + k * 4096 + 1024) = P1;                                                                   \
        *(LAS float*)(ast + k * 512) = alpha;                                                                        \
        ro = !more ? ro : (ro == RING - 16384 ? 0 : ro + 16384);                                                     \
        ++k; ++r;                                                                                                    \
      }
      u32x4 kf[4][4];
      f32x4 SA[4], SB[4];
      WAIT_FULL(16 * r + 20);
      KREAD()
      SCHEDB();
      KMMA(SA)
      LGKM0();
      DONE[v] = r + 2;
      ro = ro == RING - 16384 ? 0 : ro + 16384;
      fpre = 0;
#pragma unroll 1
      for (int k = 0; k < PH;) {
        KSTEP(SA, SB)
        KSTEP(SB, SA)
        TSTAMP2();
        if (JB <= PH) { EXCHANGE() }
      }
#define RESCALE()                                                                                                    \
        if (__builtin_amdgcn_ballot_w64(alpha != 1.0f) != 0) {                                   \
          _Pragma("unroll") for (int e = 0; e < 8; ++e) O[e] *= alpha;                                              \
        }                                                                                                            \
        asm volatile("" : "+v"(O[0]), "+v"(O[1]), "+v"(O[2]), "+v"(O[3]), "+v"(O[4]), "+v"(O[5]), "+v"(O[6]), "+v"(O[7]));
      P0 = *(const LAS u32x4*)(pst);
      P1 = *(const LAS u32x4*)(pst + 1024);
      alpha = *(const LAS float*)(ast);
#pragma unroll 1
      for (int k = 1; k <= PH - half; ++k, ++r) {
        RESCALE()
        WAIT_FULL(16 * r + 20);
        VREAD(vf, ro)
        SCHEDB();
        VREAD(vg, ro + 8192)
        const u32x4 P0n = *(const LAS u32x4*)(pst + (k & (PH - 1)) * 4096), P1n = *(const LAS u32x4*)(pst + (k & (PH - 1)) * 4096 + 1024);
        alpha = *(const LAS float*)(ast + (k & (PH - 1)) * 512);
        fpre = *FULL;
        SCHEDB();
        PVMMA(vf, P0)
        PVMMA(vg, P1)
        SCHEDB();
        LGKM0();
        DONE[v] = r + 2;
        P0 = P0n;
        P1 = P1n;
        ro = ro == RING - 16384 ? 0 : ro + 16384;
        if (JB + 6 > PH && r > PH) { EXCHANGE() }
        if ((r & 1) == 1) TSTAMP2();
      }
    }
    const __amdgpu_buffer_rsrc_t ors = __builtin_amdgcn_make_buffer_rsrc((void*)(out + ((size_t)bh * M_ + 16 * v) * D_), 0, 16 * D_ * 2, 0x00020000);
    RESCALE()
    WAIT_FULL(NPIECE - 8);
    VREAD(vf, ro)
    fpre = *FULL;
    SCHEDB();
    PVMMA(vf, P0)
    SCHEDB();
    WAIT_FULL(NPIECE);
    VREAD(vg, ro + 8192)
    SCHEDB();
    PVMMA(vg, P1)
    SCHEDB();
#pragma unroll
    for (int e2 = 0; e2 < 4; ++e2) {
      const f32x4 a = O[2 * e2], b = O[2 * e2 + 1];
      __builtin_amdgcn_raw_buffer_store_b128(u32x4{pack_bf16(a[0], a[1]), pack_bf16(a[2], a[3]), pack_bf16(b[0], b[1]), pack_bf16(b[2], b[3])},
                                             ors, c * 256 + g * 16 + e2 * 64, 0, 2);
    }
  }
}
}

extern "C" void kernel_launch(void* const* d_in, const int* in_sizes, int n_in, void* d_out, int out_size, void* d_ws,
                              size_t ws_size, hipStream_t stream) {
  const u16* q = (const u16*)d_in[0];
  const u16* kc = (const u16*)d_in[1];
  const u16* vc = (const u16*)d_in[2];
  const u16* kt = (const u16*)d_in[3];
  const u16* vt = (const u16*)d_in[4];
  const u16* mask = (const u16*)d_in[5];
  (void)hipFuncSetAttribute((const void*)tree_attn_kernel, hipFuncAttributeMaxDynamicSharedMemorySize, LDS_BYTES);
  tree_attn_kernel<<<dim3(NWG), dim3(192), LDS_BYTES, stream>>>(q, kc, vc, kt, vt, mask, (u16*)d_out, (unsigned long long*)d_ws
  );
}
```

```cpp
#include <hip/hip_runtime.h>
#include <hip/hip_bf16.h>
#include <stdint.h>

#define WINL 20
#define WINB 28

namespace {
constexpr int B_ = 32, HQ = 32, HKV = 8, T_ = 8, C_ = 2048, D_ = 128;
constexpr int G_ = HQ / HKV;
constexpr int M_ = G_ * T_;
constexpr int NWG = B_ * HKV;
constexpr int PH = 16;
constexpr int NROUND = 4 * PH;
constexpr int NPIECE = NROUND * 16;
constexpr int JA = 6, JB = 14;
constexpr float PMARG = 0.994f;
constexpr unsigned DELTA = 320;
constexpr unsigned CODEPF = 9216;
static_assert(M_ == 32 && D_ == 128 && T_ == 8 && NWG == 256 && C_ == 2 * PH * 64 && WINL >= 17 && WINL <= 24 && WINB <= 32 && JB % 2 == 0, "layout");

typedef __attribute__((ext_vector_type(8))) __bf16 bf16x8;
typedef __attribute__((ext_vector_type(4))) float f32x4;
typedef __attribute__((ext_vector_type(4))) unsigned u32x4;
typedef __attribute__((ext_vector_type(2))) unsigned u32x2;
typedef __attribute__((ext_vector_type(4))) short s16x4;
typedef unsigned short u16;
#define LAS __attribute__((address_space(3)))

constexpr float LOG2E = 1.4426950408889634f;
constexpr float SCALE = 0.08838834764831845f;
constexpr float CS = SCALE * LOG2E;

constexpr int RING = 5 * 16384, TREE_OFF = 4 * 16384, PS_OFF = RING, AS_OFF = PS_OFF + PH * 4096, CTL_OFF = AS_OFF + PH * 512;
constexpr int LDS_BYTES = CTL_OFF + 64;
constexpr unsigned SPIN_CAP = 1u << 18;

__device__ __forceinline__ f32x4 mfma16(u32x4 a, u32x4 b, f32x4 c) {
  return __builtin_amdgcn_mfma_f32_16x16x32_bf16(__builtin_bit_cast(bf16x8, a), __builtin_bit_cast(bf16x8, b), c, 0, 0, 0);
}
__device__ __forceinline__ unsigned pack_bf16(float lo, float hi) {
  typedef __attribute__((ext_vector_type(2))) float f32x2;
  typedef __attribute__((ext_vector_type(2))) __bf16 bf16x2;
  f32x2 v = {lo, hi};
  bf16x2 r = __builtin_convertvector(v, bf16x2);
  return __builtin_bit_cast(unsigned, r);
}
__device__ __forceinline__ float max3(float a, float b, float c) { return fmaxf(fmaxf(a, b), c); }
__device__ __forceinline__ float exp2c(float x) { return __builtin_amdgcn_fmed3f(__builtin_amdgcn_exp2f(x), 0.f, 1.f); }
__device__ __forceinline__ float xrow_max(float x) {
  auto s = __builtin_amdgcn_permlane16_swap(__float_as_uint(x), __float_as_uint(x), false, false);
  x = fmaxf(__uint_as_float(s[0]), __uint_as_float(s[1]));
  auto t = __builtin_amdgcn_permlane32_swap(__float_as_uint(x), __float_as_uint(x), false, false);
  return fmaxf(__uint_as_float(t[0]), __uint_as_float(t[1]));
}
__device__ __forceinline__ float xrow_sum(float x) {
  auto s = __builtin_amdgcn_permlane16_swap(__float_as_uint(x), __float_as_uint(x), false, false);
  x = __uint_as_float(s[0]) + __uint_as_float(s[1]);
  auto t = __builtin_amdgcn_permlane32_swap(__float_as_uint(x), __float_as_uint(x), false, false);
  return __uint_as_float(t[0]) + __uint_as_float(t[1]);
}
__device__ __forceinline__ float wave_sum(float x) {
  x += __builtin_bit_cast(float, __builtin_amdgcn_mov_dpp(__builtin_bit_cast(int, x), 0xB1, 0xf, 0xf, true));
  x += __builtin_bit_cast(float, __builtin_amdgcn_mov_dpp(__builtin_bit_cast(int, x), 0x4E, 0xf, 0xf, true));
  x += __builtin_bit_cast(float, __builtin_amdgcn_mov_dpp(__builtin_bit_cast(int, x), 0x141, 0xf, 0xf, true));
  x += __builtin_bit_cast(float, __builtin_amdgcn_mov_dpp(__builtin_bit_cast(int, x), 0x140, 0xf, 0xf, true));
  return xrow_sum(x);
}
__device__ __forceinline__ u32x4 make_srd(const void* p, unsigned bytes) {
  const uint64_t a = (uint64_t)p;
  return u32x4{(unsigned)a, (unsigned)(a >> 32) & 0xffffu, bytes, 0x00020000u};
}
#define STR_(x) #x
#define STR(x) STR_(x)
#define SET_M0(x) asm volatile("s_mov_b32 m0, %0\n\ts_nop 0" :: "s"(x) : "memory")
#define DMAW(srd, voff, soff, IMM, W)                                                                            \
  asm volatile("buffer_load_dwordx4 %0, %1, %2 offen offset:" #IMM " nt lds\n\ts_waitcnt vmcnt(" STR(W) ")"      \
               :: "v"(voff), "s"(srd), "s"(soff) : "memory")
#define DMA(srd, voff, soff, IMM) DMAW(srd, voff, soff, IMM, WINL)
#define DMAB(srd, voff, soff, IMM) DMAW(srd, voff, soff, IMM, WINB)
#define DMA0(srd, voff, soff, IMM) DMAW(srd, voff, soff, IMM, 63)
#define MEMFENCE() asm volatile("" ::: "memory")
#define LGKM0() do { MEMFENCE(); __builtin_amdgcn_s_waitcnt(0xc07f); MEMFENCE(); } while (0)
#define SCHEDB() __builtin_amdgcn_sched_barrier(0)

__global__ __launch_bounds__(192, 2) void tree_attn_kernel(const u16* q, const u16* kc, const u16* vc, const u16* kt, const u16* vt,
                                                            const u16* mask, u16* out, unsigned long long* prog) {
  extern __shared__ __attribute__((aligned(16))) char lds_generic[];
  LAS char* lds = (LAS char*)lds_generic;
  uintptr_t pc0;
  asm volatile("s_getpc_b64 %0" : "=s"(pc0));
  const int bh = (blockIdx.x & 7) * 32 + (blockIdx.x >> 3);
  const int lane = threadIdx.x & 63;
  const int wave = __builtin_amdgcn_readfirstlane(threadIdx.x >> 6);
  const int g = lane >> 4, c = lane & 15;
  volatile LAS unsigned* FULL = (volatile LAS unsigned*)(lds + CTL_OFF);
  volatile LAS unsigned* DONE = (volatile LAS unsigned*)(lds + CTL_OFF + 16);
  volatile LAS unsigned* SCHED = (volatile LAS unsigned*)(lds + CTL_OFF + 24);

  const unsigned lds0 = (unsigned)(uintptr_t)lds;
  const size_t base = (size_t)bh * C_ * D_;
  const int kv0 = g * 256 + ((c ^ g) << 4), vv0 = g * 256 + ((c ^ (g << 2)) << 4);
  u32x4 srd = make_srd(kc + base, C_ * D_ * 2);
  int o0 = kv0, o1 = kv0 ^ 64, o2 = kv0 ^ 128, o3 = kv0 ^ 192;
#define BATCH(D, go, ro, P, i)                                                                                       \
    SET_M0(lds0 + (ro));                                                                                             \
    D(srd, o0, go, 0); P(i + 1) D(srd, o1, go, 1024); P(i + 2) D(srd, o2, go, 2048); P(i + 3) D(srd, o3, go, 3072); P(i + 4)
#define ROUND(D, go, ro, P) BATCH(D, go, ro, P, 0) BATCH(D, go + 4096, ro + 4096, P, 4) BATCH(D, go + 8192, ro + 8192, P, 8) BATCH(D, go + 12288, ro + 12288, P, 12)
#define NOPUB(i)
#define PUB(i) if ((i) == WINL - 16) *FULL = 16 * r - 16;
#define PUBB(i) if ((i) == WINB - 16) *FULL = 16 * r - 16;
  if (wave == 0) {
    ROUND(DMA0, 0, 0, NOPUB)
  }
  MEMFENCE();
  if (wave == 0) {
    __builtin_amdgcn_s_barrier();
    MEMFENCE();
    unsigned dmin = 0;
    unsigned sbase = 0, sper = 0;
    int ro = 16384;
#pragma unroll 1
    for (int r = 1; r < NROUND; ++r) {
      if ((r & (PH - 1)) == 0) {
        const bool isv = (r / PH) & 1;
        srd = make_srd((isv ? vc : kc) + base, C_ * D_ * 2);
        o0 = isv ? vv0 : kv0; o1 = isv ? vv0 : kv0 ^ 64; o2 = isv ? vv0 : kv0 ^ 128; o3 = isv ? vv0 : kv0 ^ 192;
      }
      if (__builtin_expect((int)dmin < r - 3, 0)) {
#pragma unroll 1
        for (unsigned sp = 0; sp < SPIN_CAP; ++sp) {
          dmin = __builtin_amdgcn_readfirstlane(min(DONE[0], DONE[1]));
          if ((int)dmin >= r - 3) break;
        }
        MEMFENCE();
      }
      const int go = ((r / (2 * PH)) * PH + (r & (PH - 1))) * 16384;
      if (r == JA || r == JB) FULL[r == JA ? 2 : 3] = (unsigned)__builtin_amdgcn_s_memrealtime();
      int lag = 0;
      if (sper) {
        const unsigned due = sbase + r * sper + (r / PH) * DELTA;
        lag = (int)(((unsigned)__builtin_amdgcn_s_memrealtime() << 4) - due);
#pragma unroll 1
        for (int sp = 0; sp < 64 && lag < 0; ++sp) lag = (int)(((unsigned)__builtin_amdgcn_s_memrealtime() << 4) - due);
      }
      u32x4 cw;
      if (lag > 80) {
        BATCH(DMAB, go, ro, PUBB, 0) BATCH(DMAB, go + 4096, ro + 4096, PUBB, 4) BATCH(DMAB, go + 8192, ro + 8192, PUBB, 8)
        cw = *(volatile LAS u32x4*)DONE;
        BATCH(DMAB, go + 12288, ro + 12288, PUBB, 12)
      } else {
        BATCH(DMA, go, ro, PUB, 0) BATCH(DMA, go + 4096, ro + 4096, PUB, 4) BATCH(DMA, go + 8192, ro + 8192, PUB, 8)
        cw = *(volatile LAS u32x4*)DONE;
        BATCH(DMA, go + 12288, ro + 12288, PUB, 12)
      }
      ro = ro == RING - 16384 ? 0 : ro + 16384;
      dmin = __builtin_amdgcn_readfirstlane(min(cw[0], cw[1]));
      sbase = __builtin_amdgcn_readfirstlane(cw[2]);
      sper = __builtin_amdgcn_readfirstlane(cw[3]);
    }
#define DRAIN(n) asm volatile("s_waitcnt vmcnt(" #n ")" ::: "memory"); *FULL = NPIECE - n;
    DRAIN(16) DRAIN(8) DRAIN(0)
  } else {
    const int v = wave - 1;
    u32x4 Q[4];
    {
      const __amdgpu_buffer_rsrc_t qrs = __builtin_amdgcn_make_buffer_rsrc((void*)(q + ((size_t)bh * M_ + 16 * v) * D_), 0, 16 * D_ * 2, 0x00020000);
#pragma unroll
      for (int s = 0; s < 4; ++s) Q[s] = __builtin_amdgcn_raw_buffer_load_b128(qrs, c * 256 + g * 16 + s * 64, 0, 2);
    }
    const __amdgpu_buffer_rsrc_t mrs = __builtin_amdgcn_make_buffer_rsrc((void*)(mask + (size_t)(bh / HKV) * T_ * T_), 0, T_ * T_ * 2, 0x00020000);
    u32x2 mk = __builtin_amdgcn_raw_buffer_load_b64(mrs, (c & 7) * 16 + (g & 1) * 8, 0, 2);
    {
      const u32x4 trs = make_srd((v ? vt : kt) + (size_t)bh * T_ * D_, T_ * D_ * 2);
      const int t0 = v ? vv0 : kv0, t1 = v ? vv0 : kv0 ^ 64;
      SET_M0(lds0 + TREE_OFF + v * 8192);
      DMA0(trs, t0, 0, 0); DMA0(trs, t1, 0, 1024);
    }
    *(LAS u32x4*)(lds + TREE_OFF + 8192 + 2048 + v * 1024 + lane * 16) = u32x4{0u, 0u, 0u, 0u};
    if (v == 0 && lane < 10) FULL[lane] = 0u;
    LGKM0();
    unsigned cpf0, cpf1;
    {
      const char* pcb = (const char*)(pc0 & ~(uintptr_t)255);
      const unsigned off = min(((blockIdx.x >> 3) * 2u + v) * 256u, (unsigned)(CODEPF - 256));
      asm volatile("s_load_dword %0, %2, 0x0\n\ts_load_dword %1, %2, 0x80" : "=&s"(cpf0), "=&s"(cpf1) : "s"(pcb + off));
    }
    __builtin_amdgcn_s_barrier();
    asm volatile("" : "+v"(mk) :: "memory");

    int ka[4], va[4];
#pragma unroll
    for (int s = 0; s < 4; ++s) ka[s] = c * 256 + (((4 * s + g) ^ c) << 4);
#pragma unroll
    for (int h = 0; h < 4; ++h) va[h] = (4 * g + (c >> 2)) * 256 + ((4 * (h ^ (c >> 2)) + (c & 3)) << 4);
    LAS char* pst = lds + PS_OFF + v * 2048 + lane * 16;
    LAS char* ast = lds + AS_OFF + v * 256 + lane * 4;

#define WAIT_FULL(need)                                                        \
  do {                                                                         \
    if (__builtin_amdgcn_readfirstlane(fpre) < (unsigned)(need)) {             \
      _Pragma("unroll 1") for (unsigned sp = 0; sp < SPIN_CAP; ++sp) {        \
        if (__builtin_amdgcn_readfirstlane(*FULL) >= (unsigned)(need)) break;  \
      }                                                                        \
    }                                                                          \
    MEMFENCE();                                                                \
  } while (0)
#define TRREAD(off) __builtin_bit_cast(u32x2, __builtin_amdgcn_ds_read_tr16_b64_v4i16((LAS s16x4*)(lds + (off))))
#define VREAD(vf, so)                                                                                                \
    _Pragma("unroll") for (int e = 0; e < 8; ++e) _Pragma("unroll") for (int u = 0; u < 2; ++u)                     \
        vf[e][u] = TRREAD((so) + va[e >> 1] + (e & 1) * 8 + u * 4096);
#define PVMMA(vf, P) _Pragma("unroll") for (int e = 0; e < 8; ++e) O[e] = mfma16(u32x4{vf[e][0][0], vf[e][0][1], vf[e][1][0], vf[e][1][1]}, P, O[e]);

    unsigned fpre = 0;
    float m;
    f32x4 L;
    const u32x4 ONES = {0x3f803f80u, 0x3f803f80u, 0x3f803f80u, 0x3f803f80u};
    f32x4 O[8];
    asm volatile("s_waitcnt lgkmcnt(0)" :: "s"(cpf0), "s"(cpf1) : "memory");
    asm volatile("s_waitcnt vmcnt(0)" ::: "memory");
    FULL[8 + v] = 1u;
#pragma unroll 1
    for (unsigned sp = 0; sp < SPIN_CAP; ++sp)
      if (__builtin_amdgcn_readfirstlane(FULL[9 - v]) != 0u) break;
    MEMFENCE();
    {
      f32x4 S = {0.f, 0.f, 0.f, 0.f};
#pragma unroll
      for (int s = 0; s < 4; ++s) S = mfma16(*(const LAS u32x4*)(lds + TREE_OFF + ka[s]), Q[s], S);
      const float NB = -1000000000.0f * LOG2E;
      const float mv[4] = {__uint_as_float(mk[0] << 16), __uint_as_float(mk[0] & 0xffff0000u), __uint_as_float(mk[1] << 16), __uint_as_float(mk[1] & 0xffff0000u)};
      float x[4], p[4];
#pragma unroll
      for (int i = 0; i < 4; ++i) x[i] = g < 2 ? fmaf(S[i], CS, (1.0f - mv[i]) * NB) : -INFINITY;
      m = xrow_max(fmaxf(fmaxf(x[0], x[1]), fmaxf(x[2], x[3])));
#pragma unroll
      for (int i = 0; i < 4; ++i) p[i] = __builtin_amdgcn_exp2f(x[i] - m);
      const u32x4 P = {pack_bf16(p[0], p[1]), pack_bf16(p[2], p[3]), 0u, 0u};
      L = mfma16(ONES, P, f32x4{0.f, 0.f, 0.f, 0.f});
#pragma unroll
      for (int e = 0; e < 8; ++e) {
        const u32x2 t = TRREAD(TREE_OFF + 8192 + va[e >> 1] + (e & 1) * 8);
        O[e] = mfma16(u32x4{t[0], t[1], 0u, 0u}, P, f32x4{0.f, 0.f, 0.f, 0.f});
      }
      LGKM0();
      DONE[v] = 1;
    }

#define EXCHANGE() \
        if (v == 0 && r >= JB && r <= JB + 6) { \
          if (r == JB) { \
            ta = FULL[2]; \
            tb = FULL[3]; \
            if (lane == 0) __hip_atomic_store(prog + blockIdx.x, ((unsigned long long)tb << 32) | ta, __ATOMIC_RELAXED, __HIP_MEMORY_SCOPE_AGENT); \
          } \
          if (r == JB + 4) { \
            const __amdgpu_buffer_rsrc_t prs = __builtin_amdgcn_make_buffer_rsrc((void*)prog, 0, NWG * 8, 0x00020000); \
            pe[0] = __builtin_amdgcn_raw_buffer_load_b128(prs, lane * 32, 0, 16); \
            pe[1] = __builtin_amdgcn_raw_buffer_load_b128(prs, lane * 32 + 16, 0, 16); \
          } \
          if (r == JB + 6) { \
            float n = 0.f, sa = 0.f, sb = 0.f; \
          _Pragma("unroll") \
            for (int j = 0; j < 4; ++j) { \
              const int da = (int)(pe[j >> 1][2 * (j & 1)] - ta), db = (int)(pe[j >> 1][2 * (j & 1) + 1] - tb); \
              const bool ok = db > -400 && db < 400 && db - da > -150 && db - da < 150; \
              n += ok ? 1.f : 0.f; sa += ok ? (float)da : 0.f; sb += ok ? (float)db : 0.f; \
            } \
            n = wave_sum(n); sa = wave_sum(sa); sb = wave_sum(sb); \
            if (n >= 128.f) { \
              const float rn = __builtin_amdgcn_rcpf(n); \
              const float per = ((float)(int)(tb - ta) + (sb - sa) * rn) * (16.0f * PMARG / (JB - JA)); \
              const unsigned sb0 = (tb << 4) + (unsigned)(int)(sb * rn * 16.0f - per * JB); \
              if (lane == 0) *(volatile LAS u32x2*)SCHED = u32x2{sb0, (unsigned)per}; \
            } \
          } \
        }
    unsigned ta = 0, tb = 0;
    u32x4 pe[2] = {{0u, 0u, 0u, 0u}, {0u, 0u, 0u, 0u}};
    int ro = 0, r = 0;
    u32x2 vf[8][2], vg[8][2];
    u32x4 P0, P1;
    float alpha;
#pragma unroll 1
    for (int half = 0; half < 2; ++half) {
#define KREAD()                                                                                                      \
      _Pragma("unroll") for (int s = 0; s < 4; ++s) _Pragma("unroll") for (int u = 0; u < 4; ++u)                   \
          kf[u][s] = *(const LAS u32x4*)(lds + ro + ka[s] + u * 4096);
#define KMMA(S)                                  \
      _Pragma("unroll") for (int s = 0; s < 4; ++s) _Pragma("unroll") for (int u = 0; u < 4; ++u)                   \
          S[u] = mfma16(kf[u][s], Q[s], s == 0 ? f32x4{0.f, 0.f, 0.f, 0.f} : S[u]);
#define KSTEP(S, SN)                                                                                                 \
      {                                                                                                              \
        const bool more = k + 1 < PH;     \
        WAIT_FULL(more ? 16 * r + 32 : 0);                                                                           \
        KREAD()                                                                                                      \
        fpre = *FULL;                                                                                                \
        SCHEDB();                                                                                                    \
        const float mr = max3(max3(max3(S[0][0], S[0][1], S[0][2]), max3(S[0][3], S[1][0], S[1][1]), max3(S[1][2], S[1][3], S[2][0])), \
                              max3(S[2][1], S[2][2], S[2][3]), max3(max3(S[3][0], S[3][1], S[3][2]), S[3][3], S[3][3])); \
        const float mx = fmaxf(m, xrow_max(mr) * CS);                                    \
        alpha = __builtin_amdgcn_exp2f(m - mx);                                                                      \
        m = mx;                                                                                                      \
        L *= alpha;                                                                                                  \
        LGKM0();                                                                       \
        DONE[v] = more ? r + 3 : r + 2;                                                                              \
        SCHEDB();                                                                                                    \
        KMMA(SN)                                                                                                     \
        float p[16];                                                                                                 \
        _Pragma("unroll") for (int i = 0; i < 16; ++i) p[i] = exp2c(fmaf(S[i >> 2][i & 3], CS, -mx));               \
        P0 = u32x4{pack_bf16(p[0], p[1]), pack_bf16(p[2], p[3]), pack_bf16(p[4], p[5]), pack_bf16(p[6], p[7])};      \
        P1 = u32x4{pack_bf16(p[8], p[9]), pack_bf16(p[10], p[11]), pack_bf16(p[12], p[13]), pack_bf16(p[14], p[15])}; \
        _Pragma("unroll") for (int k_ = 0; k_ < 16; ++k_) {                                                         \
          __builtin_amdgcn_sched_group_barrier(0x8, 1, 0);                                                           \
          __builtin_amdgcn_sched_group_barrier(0x402, 3, 0);                                                      \
        }                                                                                                            \
        SCHEDB();                                                                                                    \
        L = mfma16(ONES, P0, L);      \
        L = mfma16(ONES, P1, L);                                                                                     \
        if (r == 3 * PH - 1) {                    \
          const float inv = __builtin_amdgcn_rcpf(L[0]);                                                             \
          alpha *= inv;                                                                                              \
          P0 = u32x4{pack_bf16(p[0] * inv, p[1] * inv), pack_bf16(p[2] * inv, p[3] * inv), pack_bf16(p[4] * inv, p[5] * inv), pack_bf16(p[6] * inv, p[7] * inv)}; \
          P1 = u32x4{pack_bf16(p[8] * inv, p[9] * inv), pack_bf16(p[10] * inv, p[11] * inv), pack_bf16(p[12] * inv, p[13] * inv), pack_bf16(p[14] * inv, p[15] * inv)}; \
        }                                                                                                            \
        *(LAS u32x4*)(pst + k * 4096) = P0;                                                                          \
        *(LAS u32x4*)(pst + k * 4096 + 1024) = P1;                                                                   \
        *(LAS float*)(ast + k * 512) = alpha;                                                                        \
        ro = !more ? ro : (ro == RING - 16384 ? 0 : ro + 16384);                                                     \
        ++k; ++r;                                                                                                    \
      }
      u32x4 kf[4][4];
      f32x4 SA[4], SB[4];
      WAIT_FULL(16 * r + 16);
      KREAD()
      SCHEDB();
      KMMA(SA)
      LGKM0();
      DONE[v] = r + 2;
      ro = ro == RING - 16384 ? 0 : ro + 16384;
      fpre = 0;
#pragma unroll 1
      for (int k = 0; k < PH;) {
        KSTEP(SA, SB)
        KSTEP(SB, SA)
        if (JB <= PH) { EXCHANGE() }
      }
#define RESCALE()                                                                                                    \
        if (__builtin_amdgcn_ballot_w64(alpha != 1.0f) != 0) {                                   \
          _Pragma("unroll") for (int e = 0; e < 8; ++e) O[e] *= alpha;                                              \
        }                                                                                                            \
        asm volatile("" : "+v"(O[0]), "+v"(O[1]), "+v"(O[2]), "+v"(O[3]), "+v"(O[4]), "+v"(O[5]), "+v"(O[6]), "+v"(O[7]));
      P0 = *(const LAS u32x4*)(pst);
      P1 = *(const LAS u32x4*)(pst + 1024);
      alpha = *(const LAS float*)(ast);
#pragma unroll 1
      for (int k = 1; k <= PH - half; ++k, ++r) {
        RESCALE()
        WAIT_FULL(16 * r + 16);
        VREAD(vf, ro)
        SCHEDB();
        VREAD(vg, ro + 8192)
        const u32x4 P0n = *(const LAS u32x4*)(pst + (k & (PH - 1)) * 4096), P1n = *(const LAS u32x4*)(pst + (k & (PH - 1)) * 4096 + 1024);
        alpha = *(const LAS float*)(ast + (k & (PH - 1)) * 512);
        fpre = *FULL;
        SCHEDB();
        PVMMA(vf, P0)
        PVMMA(vg, P1)
        SCHEDB();
        LGKM0();
        DONE[v] = r + 2;
        P0 = P0n;
        P1 = P1n;
        ro = ro == RING - 16384 ? 0 : ro + 16384;
        if (JB + 6 > PH && r > PH) { EXCHANGE() }
      }
    }
    const __amdgpu_buffer_rsrc_t ors = __builtin_amdgcn_make_buffer_rsrc((void*)(out + ((size_t)bh * M_ + 16 * v) * D_), 0, 16 * D_ * 2, 0x00020000);
    RESCALE()
    WAIT_FULL(NPIECE - 8);
    VREAD(vf, ro)
    fpre = *FULL;
    SCHEDB();
    PVMMA(vf, P0)
    SCHEDB();
    WAIT_FULL(NPIECE);
    VREAD(vg, ro + 8192)
    SCHEDB();
    PVMMA(vg, P1)
    SCHEDB();
#pragma unroll
    for (int e2 = 0; e2 < 4; ++e2) {
      const f32x4 a = O[2 * e2], b = O[2 * e2 + 1];
      __builtin_amdgcn_raw_buffer_store_b128(u32x4{pack_bf16(a[0], a[1]), pack_bf16(a[2], a[3]), pack_bf16(b[0], b[1]), pack_bf16(b[2], b[3])},
                                             ors, c * 256 + g * 16 + e2 * 64, 0, 2);
    }
  }
}
}

extern "C" void kernel_launch(void* const* d_in, const int* in_sizes, int n_in, void* d_out, int out_size, void* d_ws,
                              size_t ws_size, hipStream_t stream) {
  const u16* q = (const u16*)d_in[0];
  const u16* kc = (const u16*)d_in[1];
  const u16* vc = (const u16*)d_in[2];
  const u16* kt = (const u16*)d_in[3];
  const u16* vt = (const u16*)d_in[4];
  const u16* mask = (const u16*)d_in[5];
  (void)hipFuncSetAttribute((const void*)tree_attn_kernel, hipFuncAttributeMaxDynamicSharedMemorySize, LDS_BYTES);
  tree_attn_kernel<<<dim3(NWG), dim3(192), LDS_BYTES, stream>>>(q, kc, vc, kt, vt, mask, (u16*)d_out, (unsigned long long*)d_ws);
}
```

```cpp
#include <hip/hip_runtime.h>
#include <hip/hip_bf16.h>
#include <stdint.h>

#define WINL 20
#define WINB 32

namespace {
constexpr int B_ = 32, HQ = 32, HKV = 8, T_ = 8, C_ = 2048, D_ = 128;
constexpr int G_ = HQ / HKV;
constexpr int M_ = G_ * T_;
constexpr int NWG = B_ * HKV;
constexpr int PH = 16;
constexpr int NROUND = 4 * PH;
constexpr int NPIECE = NROUND * 16;
constexpr int JA = 6, JB = 14;
constexpr float PMARG = 0.994f;
constexpr unsigned DELTA = 320;
constexpr unsigned CODEPF = 7680;
static_assert(M_ == 32 && D_ == 128 && T_ == 8 && NWG == 256 && C_ == 2 * PH * 64 && WINL >= 17 && WINL <= 24 && WINB <= 32 && JB % 2 == 0, "layout");

typedef __attribute__((ext_vector_type(8))) __bf16 bf16x8;
typedef __attribute__((ext_vector_type(4))) float f32x4;
typedef __attribute__((ext_vector_type(2))) float f32x2;
typedef __attribute__((ext_vector_type(4))) unsigned u32x4;
typedef __attribute__((ext_vector_type(2))) unsigned u32x2;
typedef __attribute__((ext_vector_type(4))) short s16x4;
typedef unsigned short u16;
#define LAS __attribute__((address_space(3)))

constexpr float LOG2E = 1.4426950408889634f;
constexpr float SCALE = 0.08838834764831845f;
constexpr float CS = SCALE * LOG2E;

constexpr int RING = 5 * 16384, TREE_OFF = 4 * 16384, PS_OFF = RING, AS_OFF = PS_OFF + PH * 4096, XM_OFF = AS_OFF + PH * 256, CTL_OFF = XM_OFF + 512;
constexpr int LDS_BYTES = CTL_OFF + 128;
constexpr unsigned SPIN_CAP = 1u << 18;

__device__ __forceinline__ f32x4 mfma16(u32x4 a, u32x4 b, f32x4 c) {
  return __builtin_amdgcn_mfma_f32_16x16x32_bf16(__builtin_bit_cast(bf16x8, a), __builtin_bit_cast(bf16x8, b), c, 0, 0, 0);
}
__device__ __forceinline__ unsigned pack_bf16(float lo, float hi) {
  typedef __attribute__((ext_vector_type(2))) __bf16 bf16x2;
  f32x2 v = {lo, hi};
  bf16x2 r = __builtin_convertvector(v, bf16x2);
  return __builtin_bit_cast(unsigned, r);
}
__device__ __forceinline__ float max3(float a, float b, float c) { return fmaxf(fmaxf(a, b), c); }
__device__ __forceinline__ float exp2c(float x) { return __builtin_amdgcn_fmed3f(__builtin_amdgcn_exp2f(x), 0.f, 1.f); }
__device__ __forceinline__ float xrow_max(float x) {
  auto s = __builtin_amdgcn_permlane16_swap(__float_as_uint(x), __float_as_uint(x), false, false);
  x = fmaxf(__uint_as_float(s[0]), __uint_as_float(s[1]));
  auto t = __builtin_amdgcn_permlane32_swap(__float_as_uint(x), __float_as_uint(x), false, false);
  return fmaxf(__uint_as_float(t[0]), __uint_as_float(t[1]));
}
__device__ __forceinline__ float xrow_sum(float x) {
  auto s = __builtin_amdgcn_permlane16_swap(__float_as_uint(x), __float_as_uint(x), false, false);
  x = __uint_as_float(s[0]) + __uint_as_float(s[1]);
  auto t = __builtin_amdgcn_permlane32_swap(__float_as_uint(x), __float_as_uint(x), false, false);
  return __uint_as_float(t[0]) + __uint_as_float(t[1]);
}
__device__ __forceinline__ float wave_sum(float x) {
  x += __builtin_bit_cast(float, __builtin_amdgcn_mov_dpp(__builtin_bit_cast(int, x), 0xB1, 0xf, 0xf, true));
  x += __builtin_bit_cast(float, __builtin_amdgcn_mov_dpp(__builtin_bit_cast(int, x), 0x4E, 0xf, 0xf, true));
  x += __builtin_bit_cast(float, __builtin_amdgcn_mov_dpp(__builtin_bit_cast(int, x), 0x141, 0xf, 0xf, true));
  x += __builtin_bit_cast(float, __builtin_amdgcn_mov_dpp(__builtin_bit_cast(int, x), 0x140, 0xf, 0xf, true));
  return xrow_sum(x);
}
__device__ __forceinline__ u32x4 make_srd(const void* p, unsigned bytes) {
  const uint64_t a = (uint64_t)p;
  return u32x4{(unsigned)a, (unsigned)(a >> 32) & 0xffffu, bytes, 0x00020000u};
}
#define STR_(x) #x
#define STR(x) STR_(x)
#define SET_M0(x) asm volatile("s_mov_b32 m0, %0\n\ts_nop 0" :: "s"(x) : "memory")
#define DMAW(srd, voff, soff, IMM, W)                                                                            \
  asm volatile("buffer_load_dwordx4 %0, %1, %2 offen offset:" #IMM " nt lds\n\ts_waitcnt vmcnt(" STR(W) ")"      \
               :: "v"(voff), "s"(srd), "s"(soff) : "memory")
#define DMA(srd, voff, soff, IMM) DMAW(srd, voff, soff, IMM, WINL)
#define DMAB(srd, voff, soff, IMM) DMAW(srd, voff, soff, IMM, WINB)
#define DMA0(srd, voff, soff, IMM) DMAW(srd, voff, soff, IMM, 63)
#define MEMFENCE() asm volatile("" ::: "memory")
#define LGKM0() do { MEMFENCE(); __builtin_amdgcn_s_waitcnt(0xc07f); MEMFENCE(); } while (0)
#define SCHEDB() __builtin_amdgcn_sched_barrier(0)

__global__ __launch_bounds__(320, 2) void tree_attn_kernel(const u16* q, const u16* kc, const u16* vc, const u16* kt, const u16* vt,
                                                            const u16* mask, u16* out, unsigned long long* prog) {
  extern __shared__ __attribute__((aligned(16))) char lds_generic[];
  LAS char* lds = (LAS char*)lds_generic;
  uintptr_t pc0;
  asm volatile("s_getpc_b64 %0" : "=s"(pc0));
  const int bh = (blockIdx.x & 7) * 32 + (blockIdx.x >> 3);
  const int lane = threadIdx.x & 63;
  const int wave = __builtin_amdgcn_readfirstlane(threadIdx.x >> 6);
  const int g = lane >> 4, c = lane & 15;
  volatile LAS unsigned* FULL = (volatile LAS unsigned*)(lds + CTL_OFF);
  volatile LAS unsigned* DONE = (volatile LAS unsigned*)(lds + CTL_OFF + 16);
  volatile LAS unsigned* SCHED = (volatile LAS unsigned*)(lds + CTL_OFF + 32);
  volatile LAS unsigned* FLAG = (volatile LAS unsigned*)(lds + CTL_OFF + 48);

  const unsigned lds0 = (unsigned)(uintptr_t)lds;
  const size_t base = (size_t)bh * C_ * D_;
  const int kv0 = g * 256 + ((c ^ g) << 4), vv0 = g * 256 + ((c ^ (g << 2)) << 4);
  u32x4 srd = make_srd(kc + base, C_ * D_ * 2);
  int o0 = kv0, o1 = kv0 ^ 64, o2 = kv0 ^ 128, o3 = kv0 ^ 192;
#define BATCH(D, go, ro, P, i)                                                                                       \
    SET_M0(lds0 + (ro));                                                                                             \
    D(srd, o0, go, 0); P(i + 1) D(srd, o1, go, 1024); P(i + 2) D(srd, o2, go, 2048); P(i + 3) D(srd, o3, go, 3072); P(i + 4)
#define ROUND(D, go, ro, P) BATCH(D, go, ro, P, 0) BATCH(D, go + 4096, ro + 4096, P, 4) BATCH(D, go + 8192, ro + 8192, P, 8) BATCH(D, go + 12288, ro + 12288, P, 12)
#define NOPUB(i)
#define PUB(i) if ((i) == WINL - 16) *FULL = 16 * r - 16;
#define PUBB(i) if ((i) == WINB - 16) *FULL = 16 * r - 16;
  if (wave == 0) {
    ROUND(DMA0, 0, 0, NOPUB)
  }
  MEMFENCE();
  if (wave == 0) {
    __builtin_amdgcn_s_setprio(3);
    __builtin_amdgcn_s_barrier();
    MEMFENCE();
    unsigned dmin = 0;
    unsigned sbase = 0, sper = 0;
    int ro = 16384;
#pragma unroll 1
    for (int r = 1; r < NROUND; ++r) {
      if ((r & (PH - 1)) == 0) {
        const bool isv = (r / PH) & 1;
        srd = make_srd((isv ? vc : kc) + base, C_ * D_ * 2);
        o0 = isv ? vv0 : kv0; o1 = isv ? vv0 : kv0 ^ 64; o2 = isv ? vv0 : kv0 ^ 128; o3 = isv ? vv0 : kv0 ^ 192;
      }
      if (__builtin_expect((int)dmin < r - 3, 0)) {
#pragma unroll 1
        for (unsigned sp = 0; sp < SPIN_CAP; ++sp) {
          dmin = __builtin_amdgcn_readfirstlane(min(min(DONE[0], DONE[1]), min(DONE[2], DONE[3])));
          if ((int)dmin >= r - 3) break;
        }
        MEMFENCE();
      }
      const int go = ((r / (2 * PH)) * PH + (r & (PH - 1))) * 16384;
      if (r == JA || r == JB) FULL[r == JA ? 2 : 3] = (unsigned)__builtin_amdgcn_s_memrealtime();
      int lag = 0;
      if (sper) {
        const unsigned due = sbase + r * sper + (r / PH) * DELTA;
        lag = (int)(((unsigned)__builtin_amdgcn_s_memrealtime() << 4) - due);
#pragma unroll 1
        for (int sp = 0; sp < 64 && lag < 0; ++sp) lag = (int)(((unsigned)__builtin_amdgcn_s_memrealtime() << 4) - due);
      }
      u32x4 cw;
      u32x2 sw;
      if (lag > 80) {
        BATCH(DMAB, go, ro, PUBB, 0) BATCH(DMAB, go + 4096, ro + 4096, PUBB, 4) BATCH(DMAB, go + 8192, ro + 8192, PUBB, 8)
        cw = *(volatile LAS u32x4*)DONE;
        sw = *(volatile LAS u32x2*)SCHED;
        BATCH(DMAB, go + 12288, ro + 12288, PUBB, 12)
      } else {
        BATCH(DMA, go, ro, PUB, 0) BATCH(DMA, go + 4096, ro + 4096, PUB, 4) BATCH(DMA, go + 8192, ro + 8192, PUB, 8)
        cw = *(volatile LAS u32x4*)DONE;
        sw = *(volatile LAS u32x2*)SCHED;
        BATCH(DMA, go + 12288, ro + 12288, PUB, 12)
      }
      ro = ro == RING - 16384 ? 0 : ro + 16384;
      dmin = __builtin_amdgcn_readfirstlane(min(min(cw[0], cw[1]), min(cw[2], cw[3])));
      sbase = __builtin_amdgcn_readfirstlane(sw[0]);
      sper = __builtin_amdgcn_readfirstlane(sw[1]);
    }
#define DRAIN(n) asm volatile("s_waitcnt vmcnt(" #n ")" ::: "memory"); *FULL = NPIECE - n;
    DRAIN(16) DRAIN(8) DRAIN(0)
  } else {
    const int ci = wave - 1, v = ci & 1, h = ci >> 1;
    u32x4 Q[4];
    {
      const __amdgpu_buffer_rsrc_t qrs = __builtin_amdgcn_make_buffer_rsrc((void*)(q + ((size_t)bh * M_ + 16 * v) * D_), 0, 16 * D_ * 2, 0x00020000);
#pragma unroll
      for (int s = 0; s < 4; ++s) Q[s] = __builtin_amdgcn_raw_buffer_load_b128(qrs, c * 256 + g * 16 + s * 64, 0, 2);
    }
    const __amdgpu_buffer_rsrc_t mrs = __builtin_amdgcn_make_buffer_rsrc((void*)(mask + (size_t)(bh / HKV) * T_ * T_), 0, T_ * T_ * 2, 0x00020000);
    u32x2 mk = __builtin_amdgcn_raw_buffer_load_b64(mrs, (c & 7) * 16 + (g & 1) * 8, 0, 2);
    if (h == 0) {
      const u32x4 trs = make_srd((v ? vt : kt) + (size_t)bh * T_ * D_, T_ * D_ * 2);
      const int t0 = v ? vv0 : kv0, t1 = v ? vv0 : kv0 ^ 64;
      SET_M0(lds0 + TREE_OFF + v * 8192);
      DMA0(trs, t0, 0, 0); DMA0(trs, t1, 0, 1024);
    } else {
      *(LAS u32x4*)(lds + TREE_OFF + 8192 + 2048 + v * 1024 + lane * 16) = u32x4{0u, 0u, 0u, 0u};
      if (v == 0 && lane < 32) FULL[lane] = 0u;
    }
    LGKM0();
    unsigned cpf0;
    {
      const char* pcb = (const char*)(pc0 & ~(uintptr_t)255);
      const unsigned off = min(((blockIdx.x >> 3) * 4u + ci) * 128u, (unsigned)(CODEPF - 128));
      asm volatile("s_load_dword %0, %1, 0x0" : "=&s"(cpf0) : "s"(pcb + off));
    }
    __builtin_amdgcn_s_barrier();
    asm volatile("" : "+v"(mk) :: "memory");

    int ka[4], va[2];
#pragma unroll
    for (int s = 0; s < 4; ++s) ka[s] = c * 256 + (((4 * s + g) ^ c) << 4);
#pragma unroll
    for (int j = 0; j < 2; ++j) va[j] = (4 * g + (c >> 2)) * 256 + ((4 * ((2 * h + j) ^ (c >> 2)) + (c & 3)) << 4);
    LAS char* pst = lds + PS_OFF + v * 2048 + lane * 16;
    LAS char* mst = lds + AS_OFF + v * 128 + c * 8;
    volatile LAS float* lsum = (volatile LAS float*)(lds + XM_OFF + v * 128 + c * 4);

#define WAIT_FULL(need)                                                        \
  do {                                                                         \
    if (__builtin_amdgcn_readfirstlane(fpre) < (unsigned)(need)) {             \
      _Pragma("unroll 1") for (unsigned sp = 0; sp < SPIN_CAP; ++sp) {        \
        if (__builtin_amdgcn_readfirstlane(*FULL) >= (unsigned)(need)) break;  \
      }                                                                        \
    }                                                                          \
    MEMFENCE();                                                                \
  } while (0)
#define WAIT_FLAG(i)                                                           \
  do {                                                                         \
    _Pragma("unroll 1") for (unsigned sp = 0; sp < SPIN_CAP; ++sp)            \
      if (__builtin_amdgcn_readfirstlane(FLAG[i]) != 0u) break;                \
    MEMFENCE();                                                                \
  } while (0)
#define TRREAD(off) __builtin_bit_cast(u32x2, __builtin_amdgcn_ds_read_tr16_b64_v4i16((LAS s16x4*)(lds + (off))))
#define VREAD(vf, so)                                                                                                \
    _Pragma("unroll") for (int e = 0; e < 4; ++e) _Pragma("unroll") for (int u = 0; u < 2; ++u)                     \
        vf[e][u] = TRREAD((so) + va[e >> 1] + (e & 1) * 8 + u * 4096);
#define PVMMA(vf, P) _Pragma("unroll") for (int e = 0; e < 4; ++e) O[e] = mfma16(u32x4{vf[e][0][0], vf[e][0][1], vf[e][1][0], vf[e][1][1]}, P, O[e]);

    unsigned fpre = 0;
    float m, M;
    f32x4 L = {0.f, 0.f, 0.f, 0.f};
    const u32x4 ONES = {0x3f803f80u, 0x3f803f80u, 0x3f803f80u, 0x3f803f80u};
    f32x4 O[4];
    asm volatile("s_waitcnt lgkmcnt(0)" :: "s"(cpf0) : "memory");
    {
      asm volatile("s_waitcnt vmcnt(0)" ::: "memory");
      if (h == 0) FLAG[v] = 1u;
      WAIT_FLAG(0);
      WAIT_FLAG(1);
      f32x4 S = {0.f, 0.f, 0.f, 0.f};
#pragma unroll
      for (int s = 0; s < 4; ++s) S = mfma16(*(const LAS u32x4*)(lds + TREE_OFF + ka[s]), Q[s], S);
      const float NB = -1000000000.0f * LOG2E;
      const float mv[4] = {__uint_as_float(mk[0] << 16), __uint_as_float(mk[0] & 0xffff0000u), __uint_as_float(mk[1] << 16), __uint_as_float(mk[1] & 0xffff0000u)};
      float x[4], p[4];
#pragma unroll
      for (int i = 0; i < 4; ++i) x[i] = g < 2 ? fmaf(S[i], CS, (1.0f - mv[i]) * NB) : -INFINITY;
      M = m = __builtin_ceilf(xrow_max(fmaxf(fmaxf(x[0], x[1]), fmaxf(x[2], x[3]))));
#pragma unroll
      for (int i = 0; i < 4; ++i) p[i] = __builtin_amdgcn_exp2f(x[i] - m);
      const u32x4 P = {pack_bf16(p[0], p[1]), pack_bf16(p[2], p[3]), 0u, 0u};
      if (h == 0) L = mfma16(ONES, P, L);
#pragma unroll
      for (int e = 0; e < 4; ++e) {
        const u32x2 t = TRREAD(TREE_OFF + 8192 + va[e >> 1] + (e & 1) * 8);
        O[e] = mfma16(u32x4{t[0], t[1], 0u, 0u}, P, f32x4{0.f, 0.f, 0.f, 0.f});
      }
      LGKM0();
    }
    DONE[ci] = 1;

#define EXCHANGE() \
        if (ci == 2 && r >= JB && r <= JB + 6) { \
          if (r == JB) { \
            ta = FULL[2]; \
            tb = FULL[3]; \
            if (lane == 0) __hip_atomic_store(prog + blockIdx.x, ((unsigned long long)tb << 32) | ta, __ATOMIC_RELAXED, __HIP_MEMORY_SCOPE_AGENT); \
          } \
          if (r == JB + 4) { \
            const __amdgpu_buffer_rsrc_t prs = __builtin_amdgcn_make_buffer_rsrc((void*)prog, 0, NWG * 8, 0x00020000); \
            pe[0] = __builtin_amdgcn_raw_buffer_load_b128(prs, lane * 32, 0, 16); \
            pe[1] = __builtin_amdgcn_raw_buffer_load_b128(prs, lane * 32 + 16, 0, 16); \
          } \
          if (r == JB + 6) { \
            float n = 0.f, sa = 0.f, sb = 0.f; \
          _Pragma("unroll") \
            for (int j = 0; j < 4; ++j) { \
              const int da = (int)(pe[j >> 1][2 * (j & 1)] - ta), db = (int)(pe[j >> 1][2 * (j & 1) + 1] - tb); \
              const bool ok = db > -400 && db < 400 && db - da > -150 && db - da < 150; \
              n += ok ? 1.f : 0.f; sa += ok ? (float)da : 0.f; sb += ok ? (float)db : 0.f; \
            } \
            n = wave_sum(n); sa = wave_sum(sa); sb = wave_sum(sb); \
            if (n >= 128.f) { \
              const float rn = __builtin_amdgcn_rcpf(n); \
              const float per = ((float)(int)(tb - ta) + (sb - sa) * rn) * (16.0f * PMARG / (JB - JA)); \
              const unsigned sb0 = (tb << 4) + (unsigned)(int)(sb * rn * 16.0f - per * JB); \
              if (lane == 0) *(volatile LAS u32x2*)SCHED = u32x2{sb0, (unsigned)per}; \
            } \
          } \
        }
    unsigned ta = 0, tb = 0;
    u32x4 pe[2] = {{0u, 0u, 0u, 0u}, {0u, 0u, 0u, 0u}};
    int ro = 0, r = 0;
    u32x2 vf[4][2], vg[4][2];
    u32x4 P0, P1;
    f32x2 mp;
#define NEXT_ROUND() { DONE[ci] = r + 2; ro = ro == RING - 16384 ? 0 : ro + 16384; ++r; }
#pragma unroll 1
    for (int half = 0; half < 2; ++half) {
#pragma unroll 1
      for (int k = 0; k < PH; ++k) {
        WAIT_FULL(16 * r + 16);
        u32x4 kf[2][4];
#pragma unroll
        for (int s = 0; s < 4; ++s)
#pragma unroll
          for (int u = 0; u < 2; ++u) kf[u][s] = *(const LAS u32x4*)(lds + ro + h * 8192 + ka[s] + u * 4096);
        SCHEDB();
        f32x4 S[2];
#pragma unroll
        for (int s = 0; s < 4; ++s)
#pragma unroll
          for (int u = 0; u < 2; ++u) S[u] = mfma16(kf[u][s], Q[s], s == 0 ? f32x4{0.f, 0.f, 0.f, 0.f} : S[u]);
        LGKM0();
        NEXT_ROUND()
        const float mr = max3(max3(S[0][0], S[0][1], S[0][2]), max3(S[0][3], S[1][0], S[1][1]), fmaxf(S[1][2], S[1][3]));
        const float mx = fmaxf(m, __builtin_ceilf(xrow_max(mr) * CS));
        const float alpha = __builtin_amdgcn_exp2f(m - mx);
        m = mx;
        if (g == 0) *(LAS float*)(mst + k * 256 + h * 4) = mx;
        float p[8];
#pragma unroll
        for (int i = 0; i < 8; ++i) p[i] = exp2c(fmaf(S[i >> 2][i & 3], CS, -mx));
        P0 = u32x4{pack_bf16(p[0], p[1]), pack_bf16(p[2], p[3]), pack_bf16(p[4], p[5]), pack_bf16(p[6], p[7])};
        L = mfma16(ONES, P0, L * alpha);
        *(LAS u32x4*)(pst + k * 4096 + h * 1024) = P0;
        fpre = *FULL;
        if (JB <= PH) { EXCHANGE() }
      }
      if (g == 0) lsum[16 * h] = L[0];
#define RESCALE()                                                                                                    \
        {                                                                                                            \
          const float Mn = max3(M, mp[0], mp[1]);                                                                    \
          if (__builtin_amdgcn_ballot_w64(M != Mn || mp[0] != Mn || mp[1] != Mn) != 0) {                             \
            const float a = __builtin_amdgcn_exp2f(M - Mn), s0 = __builtin_amdgcn_exp2f(mp[0] - Mn), s1 = __builtin_amdgcn_exp2f(mp[1] - Mn); \
            _Pragma("unroll") for (int e = 0; e < 4; ++e) {                                                         \
              O[e] *= a;                                                                                             \
              P0[e] = pack_bf16(__uint_as_float(P0[e] << 16) * s0, __uint_as_float(P0[e] & 0xffff0000u) * s0);       \
              P1[e] = pack_bf16(__uint_as_float(P1[e] << 16) * s1, __uint_as_float(P1[e] & 0xffff0000u) * s1);       \
            }                                                                                                        \
            M = Mn;                                                                                                  \
          }                                                                                                          \
          asm volatile("" : "+v"(O[0]), "+v"(O[1]), "+v"(O[2]), "+v"(O[3]), "+v"(P0), "+v"(P1));                     \
        }
      P0 = *(const LAS u32x4*)(pst);
      P1 = *(const LAS u32x4*)(pst + 1024);
      mp = *(const LAS f32x2*)(mst);
#pragma unroll 1
      for (int k = 1; k < PH; ++k) {
        RESCALE()
        WAIT_FULL(16 * r + 16);
        VREAD(vf, ro)
        VREAD(vg, ro + 8192)
        const u32x4 P0n = *(const LAS u32x4*)(pst + k * 4096), P1n = *(const LAS u32x4*)(pst + k * 4096 + 1024);
        mp = *(const LAS f32x2*)(mst + k * 256);
        fpre = *FULL;
        SCHEDB();
        PVMMA(vf, P0)
        PVMMA(vg, P1)
        SCHEDB();
        LGKM0();
        NEXT_ROUND()
        P0 = P0n;
        P1 = P1n;
        if (JB + 6 > PH) { EXCHANGE() }
      }
      const __amdgpu_buffer_rsrc_t ors = __builtin_amdgcn_make_buffer_rsrc((void*)(out + ((size_t)bh * M_ + 16 * v) * D_), 0, half ? 16 * D_ * 2 : 0, 0x00020000);
      RESCALE()
      float inv = __builtin_amdgcn_rcpf(fmaf(lsum[0], __builtin_amdgcn_exp2f(mp[0] - M), lsum[16] * __builtin_amdgcn_exp2f(mp[1] - M)));
      asm volatile("" : "+v"(inv));
      WAIT_FULL(16 * r + 8);
      VREAD(vf, ro)
      fpre = *FULL;
      SCHEDB();
      PVMMA(vf, P0)
      SCHEDB();
      WAIT_FULL(16 * r + 16);
      VREAD(vg, ro + 8192)
      SCHEDB();
      PVMMA(vg, P1)
      SCHEDB();
#pragma unroll
      for (int e2 = 0; e2 < 2; ++e2) {
        const f32x4 a = O[2 * e2] * inv, b = O[2 * e2 + 1] * inv;
        __builtin_amdgcn_raw_buffer_store_b128(u32x4{pack_bf16(a[0], a[1]), pack_bf16(a[2], a[3]), pack_bf16(b[0], b[1]), pack_bf16(b[2], b[3])},
                                               ors, c * 256 + g * 16 + h * 128 + e2 * 64, 0, 2);
      }
      LGKM0();
      NEXT_ROUND()
    }
  }
}
}

extern "C" void kernel_launch(void* const* d_in, const int* in_sizes, int n_in, void* d_out, int out_size, void* d_ws,
                              size_t ws_size, hipStream_t stream) {
  const u16* q = (const u16*)d_in[0];
  const u16* kc = (const u16*)d_in[1];
  const u16* vc = (const u16*)d_in[2];
  const u16* kt = (const u16*)d_in[3];
  const u16* vt = (const u16*)d_in[4];
  const u16* mask = (const u16*)d_in[5];
  (void)hipFuncSetAttribute((const void*)tree_attn_kernel, hipFuncAttributeMaxDynamicSharedMemorySize, LDS_BYTES);
  tree_attn_kernel<<<dim3(NWG), dim3(320), LDS_BYTES, stream>>>(q, kc, vc, kt, vt, mask, (u16*)d_out, (unsigned long long*)d_ws);
}
```

```cpp
#include <hip/hip_runtime.h>
#include <hip/hip_bf16.h>
#include <stdint.h>

#define WINL 20
#define WINB 32

namespace {
constexpr int B_ = 32, HQ = 32, HKV = 8, T_ = 8, C_ = 2048, D_ = 128;
constexpr int G_ = HQ / HKV;
constexpr int M_ = G_ * T_;
constexpr int NWG = B_ * HKV;
constexpr int PH = 16;
constexpr int NROUND = 4 * PH;
constexpr int NPIECE = NROUND * 16;
constexpr int JA = 6, JB = 14;
constexpr float PMARG = 0.994f;
constexpr unsigned DELTA = 320;
constexpr unsigned CODEPF = 7680;
static_assert(M_ == 32 && D_ == 128 && T_ == 8 && NWG == 256 && C_ == 2 * PH * 64 && WINL >= 17 && WINL <= 24 && WINB <= 32 && JB % 2 == 0, "layout");

typedef __attribute__((ext_vector_type(8))) __bf16 bf16x8;
typedef __attribute__((ext_vector_type(4))) float f32x4;
typedef __attribute__((ext_vector_type(2))) float f32x2;
typedef __attribute__((ext_vector_type(4))) unsigned u32x4;
typedef __attribute__((ext_vector_type(2))) unsigned u32x2;
typedef __attribute__((ext_vector_type(4))) short s16x4;
typedef unsigned short u16;
#define LAS __attribute__((address_space(3)))

constexpr float LOG2E = 1.4426950408889634f;
constexpr float SCALE = 0.08838834764831845f;
constexpr float CS = SCALE * LOG2E;

constexpr int RING = 5 * 16384, TREE_OFF = 4 * 16384, PS_OFF = RING, AS_OFF = PS_OFF + PH * 4096, XM_OFF = AS_OFF + PH * 256, CTL_OFF = XM_OFF + 512;
constexpr int LDS_BYTES = CTL_OFF + 128;
constexpr unsigned SPIN_CAP = 1u << 18;

__device__ __forceinline__ f32x4 mfma16(u32x4 a, u32x4 b, f32x4 c) {
  return __builtin_amdgcn_mfma_f32_16x16x32_bf16(__builtin_bit_cast(bf16x8, a), __builtin_bit_cast(bf16x8, b), c, 0, 0, 0);
}
__device__ __forceinline__ unsigned pack_bf16(float lo, float hi) {
  typedef __attribute__((ext_vector_type(2))) __bf16 bf16x2;
  f32x2 v = {lo, hi};
  bf16x2 r = __builtin_convertvector(v, bf16x2);
  return __builtin_bit_cast(unsigned, r);
}
__device__ __forceinline__ float max3(float a, float b, float c) { return fmaxf(fmaxf(a, b), c); }
__device__ __forceinline__ float exp2c(float x) { return __builtin_amdgcn_fmed3f(__builtin_amdgcn_exp2f(x), 0.f, 1.f); }
__device__ __forceinline__ float xrow_max(float x) {
  auto s = __builtin_amdgcn_permlane16_swap(__float_as_uint(x), __float_as_uint(x), false, false);
  x = fmaxf(__uint_as_float(s[0]), __uint_as_float(s[1]));
  auto t = __builtin_amdgcn_permlane32_swap(__float_as_uint(x), __float_as_uint(x), false, false);
  return fmaxf(__uint_as_float(t[0]), __uint_as_float(t[1]));
}
__device__ __forceinline__ float xrow_sum(float x) {
  auto s = __builtin_amdgcn_permlane16_swap(__float_as_uint(x), __float_as_uint(x), false, false);
  x = __uint_as_float(s[0]) + __uint_as_float(s[1]);
  auto t = __builtin_amdgcn_permlane32_swap(__float_as_uint(x), __float_as_uint(x), false, false);
  return __uint_as_float(t[0]) + __uint_as_float(t[1]);
}
__device__ __forceinline__ float wave_sum(float x) {
  x += __builtin_bit_cast(float, __builtin_amdgcn_mov_dpp(__builtin_bit_cast(int, x), 0xB1, 0xf, 0xf, true));
  x += __builtin_bit_cast(float, __builtin_amdgcn_mov_dpp(__builtin_bit_cast(int, x), 0x4E, 0xf, 0xf, true));
  x += __builtin_bit_cast(float, __builtin_amdgcn_mov_dpp(__builtin_bit_cast(int, x), 0x141, 0xf, 0xf, true));
  x += __builtin_bit_cast(float, __builtin_amdgcn_mov_dpp(__builtin_bit_cast(int, x), 0x140, 0xf, 0xf, true));
  return xrow_sum(x);
}
__device__ __forceinline__ u32x4 make_srd(const void* p, unsigned bytes) {
  const uint64_t a = (uint64_t)p;
  return u32x4{(unsigned)a, (unsigned)(a >> 32) & 0xffffu, bytes, 0x00020000u};
}
#define STR_(x) #x
#define STR(x) STR_(x)
#define SET_M0(x) asm volatile("s_mov_b32 m0, %0\n\ts_nop 0" :: "s"(x) : "memory")
#define DMAW(srd, voff, soff, IMM, W)                                                                            \
  asm volatile("buffer_load_dwordx4 %0, %1, %2 offen offset:" #IMM " nt lds\n\ts_waitcnt vmcnt(" STR(W) ")"      \
               :: "v"(voff), "s"(srd), "s"(soff) : "memory")
#define DMA(srd, voff, soff, IMM) DMAW(srd, voff, soff, IMM, WINL)
#define DMAB(srd, voff, soff, IMM) DMAW(srd, voff, soff, IMM, WINB)
#define DMA0(srd, voff, soff, IMM) DMAW(srd, voff, soff, IMM, 63)
#define MEMFENCE() asm volatile("" ::: "memory")
#define LGKM0() do { MEMFENCE(); __builtin_amdgcn_s_waitcnt(0xc07f); MEMFENCE(); } while (0)
#define SCHEDB() __builtin_amdgcn_sched_barrier(0)

__global__ __launch_bounds__(320, 2) void tree_attn_kernel(const u16* q, const u16* kc, const u16* vc, const u16* kt, const u16* vt,
                                                            const u16* mask, u16* out, unsigned long long* prog) {
  extern __shared__ __attribute__((aligned(16))) char lds_generic[];
  LAS char* lds = (LAS char*)lds_generic;
  uintptr_t pc0;
  asm volatile("s_getpc_b64 %0" : "=s"(pc0));
  const int bh = (blockIdx.x & 7) * 32 + (blockIdx.x >> 3);
  const int lane = threadIdx.x & 63;
  const int wave = __builtin_amdgcn_readfirstlane(threadIdx.x >> 6);
  const int g = lane >> 4, c = lane & 15;
  volatile LAS unsigned* FULL = (volatile LAS unsigned*)(lds + CTL_OFF);
  volatile LAS unsigned* DONE = (volatile LAS unsigned*)(lds + CTL_OFF + 16);
  volatile LAS unsigned* SCHED = (volatile LAS unsigned*)(lds + CTL_OFF + 32);
  volatile LAS unsigned* FLAG = (volatile LAS unsigned*)(lds + CTL_OFF + 48);

  const unsigned lds0 = (unsigned)(uintptr_t)lds;
  const size_t base = (size_t)bh * C_ * D_;
  const int kv0 = g * 256 + ((c ^ g) << 4), vv0 = g * 256 + ((c ^ (g << 2)) << 4);
  u32x4 srd = make_srd(kc + base, C_ * D_ * 2);
  int o0 = kv0, o1 = kv0 ^ 64, o2 = kv0 ^ 128, o3 = kv0 ^ 192;
#define BATCH(D, go, ro, P, i)                                                                                       \
    SET_M0(lds0 + (ro));                                                                                             \
    D(srd, o0, go, 0); P(i + 1) D(srd, o1, go, 1024); P(i + 2) D(srd, o2, go, 2048); P(i + 3) D(srd, o3, go, 3072); P(i + 4)
#define ROUND(D, go, ro, P) BATCH(D, go, ro, P, 0) BATCH(D, go + 4096, ro + 4096, P, 4) BATCH(D, go + 8192, ro + 8192, P, 8) BATCH(D, go + 12288, ro + 12288, P, 12)
#define NOPUB(i)
#define PUB(i) if ((i) == WINL - 16) *FULL = 16 * r - 16;
#define PUBB(i) if ((i) == WINB - 16) *FULL = 16 * r - 16;
  if (wave == 0) {
    ROUND(DMA0, 0, 0, NOPUB)
  }
  MEMFENCE();
  if (wave == 0) {
    __builtin_amdgcn_s_setprio(3);
    __builtin_amdgcn_s_barrier();
    MEMFENCE();
    unsigned dmin = 0;
    unsigned sbase = 0, sper = 0;
    int ro = 16384, early = 0;
#pragma unroll 1
    for (int r = 1; r < NROUND; ++r) {
      if ((r & (PH - 1)) == 0) {
        const bool isv = (r / PH) & 1;
        srd = make_srd((isv ? vc : kc) + base, C_ * D_ * 2);
        o0 = isv ? vv0 : kv0; o1 = isv ? vv0 : kv0 ^ 64; o2 = isv ? vv0 : kv0 ^ 128; o3 = isv ? vv0 : kv0 ^ 192;
      }
      if (__builtin_expect((int)dmin < r - 3, 0)) {
#pragma unroll 1
        for (unsigned sp = 0; sp < SPIN_CAP; ++sp) {
          dmin = __builtin_amdgcn_readfirstlane(min(min(DONE[0], DONE[1]), min(DONE[2], DONE[3])));
          if ((int)dmin >= r - 3) break;
        }
        MEMFENCE();
      }
      const int go = ((r / (2 * PH)) * PH + (r & (PH - 1))) * 16384;
      if (r == JA || r == JB) FULL[r == JA ? 2 : 3] = (unsigned)__builtin_amdgcn_s_memrealtime();
      int lag = 0;
      if (sper && early < 5) {
        const unsigned due = sbase + r * sper + (r / PH) * DELTA;
        lag = (int)(((unsigned)__builtin_amdgcn_s_memrealtime() << 4) - due);
        early = lag < -(int)(sper >> 2) ? early + 1 : 0;
#pragma unroll 1
        for (int sp = 0; sp < 64 && lag < 0; ++sp) lag = (int)(((unsigned)__builtin_amdgcn_s_memrealtime() << 4) - due);
      }
      u32x4 cw;
      u32x2 sw;
      if (lag > 80) {
        BATCH(DMAB, go, ro, PUBB, 0) BATCH(DMAB, go + 4096, ro + 4096, PUBB, 4) BATCH(DMAB, go + 8192, ro + 8192, PUBB, 8)
        cw = *(volatile LAS u32x4*)DONE;
        sw = *(volatile LAS u32x2*)SCHED;
        BATCH(DMAB, go + 12288, ro + 12288, PUBB, 12)
      } else {
        BATCH(DMA, go, ro, PUB, 0) BATCH(DMA, go + 4096, ro + 4096, PUB, 4) BATCH(DMA, go + 8192, ro + 8192, PUB, 8)
        cw = *(volatile LAS u32x4*)DONE;
        sw = *(volatile LAS u32x2*)SCHED;
        BATCH(DMA, go + 12288, ro + 12288, PUB, 12)
      }
      ro = ro == RING - 16384 ? 0 : ro + 16384;
      dmin = __builtin_amdgcn_readfirstlane(min(min(cw[0], cw[1]), min(cw[2], cw[3])));
      sbase = __builtin_amdgcn_readfirstlane(sw[0]);
      sper = __builtin_amdgcn_readfirstlane(sw[1]);
    }
#define DRAIN(n) asm volatile("s_waitcnt vmcnt(" #n ")" ::: "memory"); *FULL = NPIECE - n;
    DRAIN(16) DRAIN(8) DRAIN(0)
  } else {
    const int ci = wave - 1, v = ci & 1, h = ci >> 1;
    u32x4 Q[4];
    {
      const __amdgpu_buffer_rsrc_t qrs = __builtin_amdgcn_make_buffer_rsrc((void*)(q + ((size_t)bh * M_ + 16 * v) * D_), 0, 16 * D_ * 2, 0x00020000);
#pragma unroll
      for (int s = 0; s < 4; ++s) Q[s] = __builtin_amdgcn_raw_buffer_load_b128(qrs, c * 256 + g * 16 + s * 64, 0, 2);
    }
    const __amdgpu_buffer_rsrc_t mrs = __builtin_amdgcn_make_buffer_rsrc((void*)(mask + (size_t)(bh / HKV) * T_ * T_), 0, T_ * T_ * 2, 0x00020000);
    u32x2 mk = __builtin_amdgcn_raw_buffer_load_b64(mrs, (c & 7) * 16 + (g & 1) * 8, 0, 2);
    if (h == 0) {
      const u32x4 trs = make_srd((v ? vt : kt) + (size_t)bh * T_ * D_, T_ * D_ * 2);
      const int t0 = v ? vv0 : kv0, t1 = v ? vv0 : kv0 ^ 64;
      SET_M0(lds0 + TREE_OFF + v * 8192);
      DMA0(trs, t0, 0, 0); DMA0(trs, t1, 0, 1024);
    } else {
      *(LAS u32x4*)(lds + TREE_OFF + 8192 + 2048 + v * 1024 + lane * 16) = u32x4{0u, 0u, 0u, 0u};
      if (v == 0 && lane < 32) FULL[lane] = 0u;
    }
    LGKM0();
    unsigned cpf0;
    {
      const char* pcb = (const char*)(pc0 & ~(uintptr_t)255);
      const unsigned off = min(((blockIdx.x >> 3) * 4u + ci) * 128u, (unsigned)(CODEPF - 128));
      asm volatile("s_load_dword %0, %1, 0x0" : "=&s"(cpf0) : "s"(pcb + off));
    }
    __builtin_amdgcn_s_barrier();
    asm volatile("" : "+v"(mk) :: "memory");

    int ka[4], va[2];
#pragma unroll
    for (int s = 0; s < 4; ++s) ka[s] = c * 256 + (((4 * s + g) ^ c) << 4);
#pragma unroll
    for (int j = 0; j < 2; ++j) va[j] = (4 * g + (c >> 2)) * 256 + ((4 * ((2 * h + j) ^ (c >> 2)) + (c & 3)) << 4);
    LAS char* pst = lds + PS_OFF + v * 2048 + lane * 16;
    LAS char* mst = lds + AS_OFF + v * 128 + c * 8;
    volatile LAS float* lsum = (volatile LAS float*)(lds + XM_OFF + v * 128 + c * 4);

#define WAIT_FULL(need)                                                        \
  do {                                                                         \
    if (__builtin_amdgcn_readfirstlane(fpre) < (unsigned)(need)) {             \
      _Pragma("unroll 1") for (unsigned sp = 0; sp < SPIN_CAP; ++sp) {        \
        if (__builtin_amdgcn_readfirstlane(*FULL) >= (unsigned)(need)) break;  \
      }                                                                        \
    }                                                                          \
    MEMFENCE();                                                                \
  } while (0)
#define WAIT_FLAG(i)                                                           \
  do {                                                                         \
    _Pragma("unroll 1") for (unsigned sp = 0; sp < SPIN_CAP; ++sp)            \
      if (__builtin_amdgcn_readfirstlane(FLAG[i]) != 0u) break;                \
    MEMFENCE();                                                                \
  } while (0)
#define TRREAD(off) __builtin_bit_cast(u32x2, __builtin_amdgcn_ds_read_tr16_b64_v4i16((LAS s16x4*)(lds + (off))))
#define VREAD(vf, so)                                                                                                \
    _Pragma("unroll") for (int e = 0; e < 4; ++e) _Pragma("unroll") for (int u = 0; u < 2; ++u)                     \
        vf[e][u] = TRREAD((so) + va[e >> 1] + (e & 1) * 8 + u * 4096);
#define PVMMA(vf, P) _Pragma("unroll") for (int e = 0; e < 4; ++e) O[e] = mfma16(u32x4{vf[e][0][0], vf[e][0][1], vf[e][1][0], vf[e][1][1]}, P, O[e]);

    unsigned fpre = 0;
    float m, M;
    f32x4 L = {0.f, 0.f, 0.f, 0.f};
    const u32x4 ONES = {0x3f803f80u, 0x3f803f80u, 0x3f803f80u, 0x3f803f80u};
    f32x4 O[4];
    asm volatile("s_waitcnt lgkmcnt(0)" :: "s"(cpf0) : "memory");
    {
      asm volatile("s_waitcnt vmcnt(0)" ::: "memory");
      if (h == 0) FLAG[v] = 1u;
      WAIT_FLAG(0);
      WAIT_FLAG(1);
      f32x4 S = {0.f, 0.f, 0.f, 0.f};
#pragma unroll
      for (int s = 0; s < 4; ++s) S = mfma16(*(const LAS u32x4*)(lds + TREE_OFF + ka[s]), Q[s], S);
      const float NB = -1000000000.0f * LOG2E;
      const float mv[4] = {__uint_as_float(mk[0] << 16), __uint_as_float(mk[0] & 0xffff0000u), __uint_as_float(mk[1] << 16), __uint_as_float(mk[1] & 0xffff0000u)};
      float x[4], p[4];
#pragma unroll
      for (int i = 0; i < 4; ++i) x[i] = g < 2 ? fmaf(S[i], CS, (1.0f - mv[i]) * NB) : -INFINITY;
      M = m = __builtin_ceilf(xrow_max(fmaxf(fmaxf(x[0], x[1]), fmaxf(x[2], x[3]))));
#pragma unroll
      for (int i = 0; i < 4; ++i) p[i] = __builtin_amdgcn_exp2f(x[i] - m);
      const u32x4 P = {pack_bf16(p[0], p[1]), pack_bf16(p[2], p[3]), 0u, 0u};
      if (h == 0) L = mfma16(ONES, P, L);
#pragma unroll
      for (int e = 0; e < 4; ++e) {
        const u32x2 t = TRREAD(TREE_OFF + 8192 + va[e >> 1] + (e & 1) * 8);
        O[e] = mfma16(u32x4{t[0], t[1], 0u, 0u}, P, f32x4{0.f, 0.f, 0.f, 0.f});
      }
      LGKM0();
    }
    DONE[ci] = 1;

#define EXCHANGE() \
        if (ci == 2 && r >= JB && r <= JB + 6) { \
          if (r == JB) { \
            ta = FULL[2]; \
            tb = FULL[3]; \
            if (lane == 0) __hip_atomic_store(prog + blockIdx.x, ((unsigned long long)tb << 32) | ta, __ATOMIC_RELAXED, __HIP_MEMORY_SCOPE_AGENT); \
          } \
          if (r == JB + 4) { \
            const __amdgpu_buffer_rsrc_t prs = __builtin_amdgcn_make_buffer_rsrc((void*)prog, 0, NWG * 8, 0x00020000); \
            pe[0] = __builtin_amdgcn_raw_buffer_load_b128(prs, lane * 32, 0, 16); \
            pe[1] = __builtin_amdgcn_raw_buffer_load_b128(prs, lane * 32 + 16, 0, 16); \
          } \
          if (r == JB + 6) { \
            float n = 0.f, sa = 0.f, sb = 0.f; \
          _Pragma("unroll") \
            for (int j = 0; j < 4; ++j) { \
              const int da = (int)(pe[j >> 1][2 * (j & 1)] - ta), db = (int)(pe[j >> 1][2 * (j & 1) + 1] - tb); \
              const bool ok = db > -400 && db < 400 && db - da > -150 && db - da < 150; \
              n += ok ? 1.f : 0.f; sa += ok ? (float)da : 0.f; sb += ok ? (float)db : 0.f; \
            } \
            n = wave_sum(n); sa = wave_sum(sa); sb = wave_sum(sb); \
            if (n >= 128.f) { \
              const float rn = __builtin_amdgcn_rcpf(n); \
              const float per = ((float)(int)(tb - ta) + (sb - sa) * rn) * (16.0f * PMARG / (JB - JA)); \
              const unsigned sb0 = (tb << 4) + (unsigned)(int)(sb * rn * 16.0f - per * JB); \
              if (lane == 0) *(volatile LAS u32x2*)SCHED = u32x2{sb0, (unsigned)per}; \
            } \
          } \
        }
    unsigned ta = 0, tb = 0;
    u32x4 pe[2] = {{0u, 0u, 0u, 0u}, {0u, 0u, 0u, 0u}};
    int ro = 0, r = 0;
    u32x2 vf[4][2], vg[4][2];
    u32x4 P0, P1;
    f32x2 mp;
#define NEXT_ROUND() { DONE[ci] = r + 2; ro = ro == RING - 16384 ? 0 : ro + 16384; ++r; }
#pragma unroll 1
    for (int half = 0; half < 2; ++half) {
#pragma unroll 1
      for (int k = 0; k < PH; ++k) {
        WAIT_FULL(16 * r + 16);
        u32x4 kf[2][4];
#pragma unroll
        for (int s = 0; s < 4; ++s)
#pragma unroll
          for (int u = 0; u < 2; ++u) kf[u][s] = *(const LAS u32x4*)(lds + ro + h * 8192 + ka[s] + u * 4096);
        SCHEDB();
        f32x4 S[2];
#pragma unroll
        for (int s = 0; s < 4; ++s)
#pragma unroll
          for (int u = 0; u < 2; ++u) S[u] = mfma16(kf[u][s], Q[s], s == 0 ? f32x4{0.f, 0.f, 0.f, 0.f} : S[u]);
        LGKM0();
        NEXT_ROUND()
        const float mr = max3(max3(S[0][0], S[0][1], S[0][2]), max3(S[0][3], S[1][0], S[1][1]), fmaxf(S[1][2], S[1][3]));
        const float mx = fmaxf(m, __builtin_ceilf(xrow_max(mr) * CS));
        const float alpha = __builtin_amdgcn_exp2f(m - mx);
        m = mx;
        if (g == 0) *(LAS float*)(mst + k * 256 + h * 4) = mx;
        float p[8];
#pragma unroll
        for (int i = 0; i < 8; ++i) p[i] = exp2c(fmaf(S[i >> 2][i & 3], CS, -mx));
        P0 = u32x4{pack_bf16(p[0], p[1]), pack_bf16(p[2], p[3]), pack_bf16(p[4], p[5]), pack_bf16(p[6], p[7])};
        L = mfma16(ONES, P0, L * alpha);
        *(LAS u32x4*)(pst + k * 4096 + h * 1024) = P0;
        fpre = *FULL;
        if (JB <= PH) { EXCHANGE() }
      }
      if (g == 0) lsum[16 * h] = L[0];
#define RESCALE()                                                                                                    \
        {                                                                                                            \
          const float Mn = max3(M, mp[0], mp[1]);                                                                    \
          if (__builtin_amdgcn_ballot_w64(M != Mn || mp[0] != Mn || mp[1] != Mn) != 0) {                             \
            const float a = __builtin_amdgcn_exp2f(M - Mn), s0 = __builtin_amdgcn_exp2f(mp[0] - Mn), s1 = __builtin_amdgcn_exp2f(mp[1] - Mn); \
            _Pragma("unroll") for (int e = 0; e < 4; ++e) {                                                         \
              O[e] *= a;                                                                                             \
              P0[e] = pack_bf16(__uint_as_float(P0[e] << 16) * s0, __uint_as_float(P0[e] & 0xffff0000u) * s0);       \
              P1[e] = pack_bf16(__uint_as_float(P1[e] << 16) * s1, __uint_as_float(P1[e] & 0xffff0000u) * s1);       \
            }                                                                                                        \
            M = Mn;                                                                                                  \
          }                                                                                                          \
          asm volatile("" : "+v"(O[0]), "+v"(O[1]), "+v"(O[2]), "+v"(O[3]), "+v"(P0), "+v"(P1));                     \
        }
      P0 = *(const LAS u32x4*)(pst);
      P1 = *(const LAS u32x4*)(pst + 1024);
      mp = *(const LAS f32x2*)(mst);
#pragma unroll 1
      for (int k = 1; k < PH; ++k) {
        RESCALE()
        WAIT_FULL(16 * r + 16);
        VREAD(vf, ro)
        VREAD(vg, ro + 8192)
        const u32x4 P0n = *(const LAS u32x4*)(pst + k * 4096), P1n = *(const LAS u32x4*)(pst + k * 4096 + 1024);
        mp = *(const LAS f32x2*)(mst + k * 256);
        fpre = *FULL;
        SCHEDB();
        PVMMA(vf, P0)
        PVMMA(vg, P1)
        SCHEDB();
        LGKM0();
        NEXT_ROUND()
        P0 = P0n;
        P1 = P1n;
        if (JB + 6 > PH) { EXCHANGE() }
      }
      const __amdgpu_buffer_rsrc_t ors = __builtin_amdgcn_make_buffer_rsrc((void*)(out + ((size_t)bh * M_ + 16 * v) * D_), 0, half ? 16 * D_ * 2 : 0, 0x00020000);
      RESCALE()
      float inv = __builtin_amdgcn_rcpf(fmaf(lsum[0], __builtin_amdgcn_exp2f(mp[0] - M), lsum[16] * __builtin_amdgcn_exp2f(mp[1] - M)));
      asm volatile("" : "+v"(inv));
      WAIT_FULL(16 * r + 8);
      VREAD(vf, ro)
      fpre = *FULL;
      SCHEDB();
      PVMMA(vf, P0)
      SCHEDB();
      WAIT_FULL(16 * r + 16);
      VREAD(vg, ro + 8192)
      SCHEDB();
      PVMMA(vg, P1)
      SCHEDB();
#pragma unroll
      for (int e2 = 0; e2 < 2; ++e2) {
        const f32x4 a = O[2 * e2] * inv, b = O[2 * e2 + 1] * inv;
        __builtin_amdgcn_raw_buffer_store_b128(u32x4{pack_bf16(a[0], a[1]), pack_bf16(a[2], a[3]), pack_bf16(b[0], b[1]), pack_bf16(b[2], b[3])},
                                               ors, c * 256 + g * 16 + h * 128 + e2 * 64, 0, 16);
      }
      LGKM0();
      NEXT_ROUND()
    }
  }
}
}

extern "C" void kernel_launch(void* const* d_in, const int* in_sizes, int n_in, void* d_out, int out_size, void* d_ws,
                              size_t ws_size, hipStream_t stream) {
  const u16* q = (const u16*)d_in[0];
  const u16* kc = (const u16*)d_in[1];
  const u16* vc = (const u16*)d_in[2];
  const u16* kt = (const u16*)d_in[3];
  const u16* vt = (const u16*)d_in[4];
  const u16* mask = (const u16*)d_in[5];
  (void)hipFuncSetAttribute((const void*)tree_attn_kernel, hipFuncAttributeMaxDynamicSharedMemorySize, LDS_BYTES);
  tree_attn_kernel<<<dim3(NWG), dim3(320), LDS_BYTES, stream>>>(q, kc, vc, kt, vt, mask, (u16*)d_out, (unsigned long long*)d_ws);
}
```

```cpp
#include <hip/hip_runtime.h>
#include <hip/hip_bf16.h>
#include <stdint.h>

#define WINL 20
#define WINB 32

namespace {
constexpr int B_ = 32, HQ = 32, HKV = 8, T_ = 8, C_ = 2048, D_ = 128;
constexpr int G_ = HQ / HKV;
constexpr int M_ = G_ * T_;
constexpr int NWG = B_ * HKV;
constexpr int PH = 16;
constexpr int NROUND = 4 * PH;
constexpr int NPIECE = NROUND * 16;
constexpr int JA = 6, JB = 14;
constexpr float PMARG = 0.994f;
constexpr unsigned DELTA = 320;
constexpr unsigned CODEPF = 7936;
static_assert(M_ == 32 && D_ == 128 && T_ == 8 && NWG == 256 && C_ == 2 * PH * 64 && WINL >= 17 && WINL <= 24 && WINB <= 32 && JB % 2 == 0, "layout");

typedef __attribute__((ext_vector_type(8))) __bf16 bf16x8;
typedef __attribute__((ext_vector_type(4))) float f32x4;
typedef __attribute__((ext_vector_type(2))) float f32x2;
typedef __attribute__((ext_vector_type(4))) unsigned u32x4;
typedef __attribute__((ext_vector_type(2))) unsigned u32x2;
typedef __attribute__((ext_vector_type(4))) short s16x4;
typedef unsigned short u16;
#define LAS __attribute__((address_space(3)))

constexpr float LOG2E = 1.4426950408889634f;
constexpr float SCALE = 0.08838834764831845f;
constexpr float CS = SCALE * LOG2E;

constexpr int RING = 5 * 16384, TREE_OFF = 4 * 16384, PS_OFF = RING, AS_OFF = PS_OFF + PH * 4096, XM_OFF = AS_OFF + PH * 256, CTL_OFF = XM_OFF + 512;
constexpr int LDS_BYTES = CTL_OFF + 128;
constexpr unsigned SPIN_CAP = 1u << 18;

__device__ __forceinline__ f32x4 mfma16(u32x4 a, u32x4 b, f32x4 c) {
  return __builtin_amdgcn_mfma_f32_16x16x32_bf16(__builtin_bit_cast(bf16x8, a), __builtin_bit_cast(bf16x8, b), c, 0, 0, 0);
}
__device__ __forceinline__ unsigned pack_bf16(float lo, float hi) {
  typedef __attribute__((ext_vector_type(2))) __bf16 bf16x2;
  f32x2 v = {lo, hi};
  bf16x2 r = __builtin_convertvector(v, bf16x2);
  return __builtin_bit_cast(unsigned, r);
}
__device__ __forceinline__ float max3(float a, float b, float c) { return fmaxf(fmaxf(a, b), c); }
__device__ __forceinline__ float exp2c(float x) { return __builtin_amdgcn_fmed3f(__builtin_amdgcn_exp2f(x), 0.f, 1.f); }
__device__ __forceinline__ float xrow_max(float x) {
  auto s = __builtin_amdgcn_permlane16_swap(__float_as_uint(x), __float_as_uint(x), false, false);
  x = fmaxf(__uint_as_float(s[0]), __uint_as_float(s[1]));
  auto t = __builtin_amdgcn_permlane32_swap(__float_as_uint(x), __float_as_uint(x), false, false);
  return fmaxf(__uint_as_float(t[0]), __uint_as_float(t[1]));
}
__device__ __forceinline__ float xrow_sum(float x) {
  auto s = __builtin_amdgcn_permlane16_swap(__float_as_uint(x), __float_as_uint(x), false, false);
  x = __uint_as_float(s[0]) + __uint_as_float(s[1]);
  auto t = __builtin_amdgcn_permlane32_swap(__float_as_uint(x), __float_as_uint(x), false, false);
  return __uint_as_float(t[0]) + __uint_as_float(t[1]);
}
__device__ __forceinline__ float wave_sum(float x) {
  x += __builtin_bit_cast(float, __builtin_amdgcn_mov_dpp(__builtin_bit_cast(int, x), 0xB1, 0xf, 0xf, true));
  x += __builtin_bit_cast(float, __builtin_amdgcn_mov_dpp(__builtin_bit_cast(int, x), 0x4E, 0xf, 0xf, true));
  x += __builtin_bit_cast(float, __builtin_amdgcn_mov_dpp(__builtin_bit_cast(int, x), 0x141, 0xf, 0xf, true));
  x += __builtin_bit_cast(float, __builtin_amdgcn_mov_dpp(__builtin_bit_cast(int, x), 0x140, 0xf, 0xf, true));
  return xrow_sum(x);
}
__device__ __forceinline__ u32x4 make_srd(const void* p, unsigned bytes) {
  const uint64_t a = (uint64_t)p;
  return u32x4{(unsigned)a, (unsigned)(a >> 32) & 0xffffu, bytes, 0x00020000u};
}
#define STR_(x) #x
#define STR(x) STR_(x)
#define SET_M0(x) asm volatile("s_mov_b32 m0, %0\n\ts_nop 0" :: "s"(x) : "memory")
#define DMAW(srd, voff, soff, IMM, W)                                                                            \
  asm volatile("buffer_load_dwordx4 %0, %1, %2 offen offset:" #IMM " nt lds\n\ts_waitcnt vmcnt(" STR(W) ")"      \
               :: "v"(voff), "s"(srd), "s"(soff) : "memory")
#define DMA(srd, voff, soff, IMM) DMAW(srd, voff, soff, IMM, WINL)
#define DMAB(srd, voff, soff, IMM) DMAW(srd, voff, soff, IMM, WINB)
#define DMA0(srd, voff, soff, IMM) DMAW(srd, voff, soff, IMM, 63)
#define MEMFENCE() asm volatile("" ::: "memory")
#define LGKM0() do { MEMFENCE(); __builtin_amdgcn_s_waitcnt(0xc07f); MEMFENCE(); } while (0)
#define SCHEDB() __builtin_amdgcn_sched_barrier(0)

__global__ __launch_bounds__(320, 2) void tree_attn_kernel(const u16* q, const u16* kc, const u16* vc, const u16* kt, const u16* vt,
                                                            const u16* mask, u16* out, unsigned long long* prog) {
  extern __shared__ __attribute__((aligned(16))) char lds_generic[];
  LAS char* lds = (LAS char*)lds_generic;
  uintptr_t pc0;
  asm volatile("s_getpc_b64 %0" : "=s"(pc0));
  const int bh = (blockIdx.x & 7) * 32 + (blockIdx.x >> 3);
  const int lane = threadIdx.x & 63;
  const int wave = __builtin_amdgcn_readfirstlane(threadIdx.x >> 6);
  const int g = lane >> 4, c = lane & 15;
  volatile LAS unsigned* FULL = (volatile LAS unsigned*)(lds + CTL_OFF);
  volatile LAS unsigned* DONE = (volatile LAS unsigned*)(lds + CTL_OFF + 16);
  volatile LAS unsigned* SCHED = (volatile LAS unsigned*)(lds + CTL_OFF + 32);
  volatile LAS unsigned* FLAG = (volatile LAS unsigned*)(lds + CTL_OFF + 48);

  const unsigned lds0 = (unsigned)(uintptr_t)lds;
  const size_t base = (size_t)bh * C_ * D_;
  const int kv0 = g * 256 + ((c ^ g) << 4), vv0 = g * 256 + ((c ^ (g << 2)) << 4);
  u32x4 srd = make_srd(kc + base, C_ * D_ * 2);
  int o0 = kv0, o1 = kv0 ^ 64, o2 = kv0 ^ 128, o3 = kv0 ^ 192;
#define BATCH(D, go, ro, P, i)                                                                                       \
    SET_M0(lds0 + (ro));                                                                                             \
    D(srd, o0, go, 0); P(i + 1) D(srd, o1, go, 1024); P(i + 2) D(srd, o2, go, 2048); P(i + 3) D(srd, o3, go, 3072); P(i + 4)
#define ROUND(D, go, ro, P) BATCH(D, go, ro, P, 0) BATCH(D, go + 4096, ro + 4096, P, 4) BATCH(D, go + 8192, ro + 8192, P, 8) BATCH(D, go + 12288, ro + 12288, P, 12)
#define NOPUB(i)
#define PUB(i) if ((i) == WINL - 16) *FULL = 16 * r - 16;
#define PUBB(i) if ((i) == WINB - 16) *FULL = 16 * r - 16;
  if (wave == 0) {
    ROUND(DMA0, 0, 0, NOPUB)
  }
  MEMFENCE();
  if (wave == 0) {
    __builtin_amdgcn_s_setprio(3);
    __builtin_amdgcn_s_barrier();
    MEMFENCE();
    unsigned dmin = 0;
    unsigned sbase = 0, sper = 0;
    int ro = 16384, early = 0, plag = 1 << 24;
    unsigned adj = 0;
#pragma unroll 1
    for (int r = 1; r < NROUND; ++r) {
      if ((r & (PH - 1)) == 0) {
        const bool isv = (r / PH) & 1;
        srd = make_srd((isv ? vc : kc) + base, C_ * D_ * 2);
        o0 = isv ? vv0 : kv0; o1 = isv ? vv0 : kv0 ^ 64; o2 = isv ? vv0 : kv0 ^ 128; o3 = isv ? vv0 : kv0 ^ 192;
      }
      if (__builtin_expect((int)dmin < r - 3, 0)) {
#pragma unroll 1
        for (unsigned sp = 0; sp < SPIN_CAP; ++sp) {
          dmin = __builtin_amdgcn_readfirstlane(min(min(DONE[0], DONE[1]), min(DONE[2], DONE[3])));
          if ((int)dmin >= r - 3) break;
        }
        MEMFENCE();
      }
      const int go = ((r / (2 * PH)) * PH + (r & (PH - 1))) * 16384;
      int lag = 0;
      if (sper && early < 5) {
        const unsigned due = sbase + r * sper + (r / PH) * DELTA + adj;
        lag = (int)(((unsigned)__builtin_amdgcn_s_memrealtime() << 4) - due);
        early = lag < -(int)(sper >> 2) ? early + 1 : 0;
#pragma unroll 1
        for (int sp = 0; sp < 64 && lag < 0; ++sp) lag = (int)(((unsigned)__builtin_amdgcn_s_memrealtime() << 4) - due);
        if (lag - plag > 1600) { adj += lag - plag; lag = plag; }
        plag = lag;
      }
      u32x4 cw;
      u32x2 sw;
      if (lag > 80) {
        BATCH(DMAB, go, ro, PUBB, 0) BATCH(DMAB, go + 4096, ro + 4096, PUBB, 4) BATCH(DMAB, go + 8192, ro + 8192, PUBB, 8)
        cw = *(volatile LAS u32x4*)DONE;
        sw = *(volatile LAS u32x2*)SCHED;
        BATCH(DMAB, go + 12288, ro + 12288, PUBB, 12)
      } else {
        BATCH(DMA, go, ro, PUB, 0)
        if (r == JA || r == JB) FULL[r == JA ? 2 : 3] = (unsigned)__builtin_amdgcn_s_memrealtime();
        BATCH(DMA, go + 4096, ro + 4096, PUB, 4) BATCH(DMA, go + 8192, ro + 8192, PUB, 8)
        cw = *(volatile LAS u32x4*)DONE;
        sw = *(volatile LAS u32x2*)SCHED;
        BATCH(DMA, go + 12288, ro + 12288, PUB, 12)
      }
      ro = ro == RING - 16384 ? 0 : ro + 16384;
      dmin = __builtin_amdgcn_readfirstlane(min(min(cw[0], cw[1]), min(cw[2], cw[3])));
      sbase = __builtin_amdgcn_readfirstlane(sw[0]);
      sper = __builtin_amdgcn_readfirstlane(sw[1]);
    }
#define DRAIN(n) asm volatile("s_waitcnt vmcnt(" #n ")" ::: "memory"); *FULL = NPIECE - n;
    DRAIN(16) DRAIN(8) DRAIN(0)
  } else {
    const int ci = wave - 1, v = ci & 1, h = ci >> 1;
    u32x4 Q[4];
    {
      const __amdgpu_buffer_rsrc_t qrs = __builtin_amdgcn_make_buffer_rsrc((void*)(q + ((size_t)bh * M_ + 16 * v) * D_), 0, 16 * D_ * 2, 0x00020000);
#pragma unroll
      for (int s = 0; s < 4; ++s) Q[s] = __builtin_amdgcn_raw_buffer_load_b128(qrs, c * 256 + g * 16 + s * 64, 0, 2);
    }
    const __amdgpu_buffer_rsrc_t mrs = __builtin_amdgcn_make_buffer_rsrc((void*)(mask + (size_t)(bh / HKV) * T_ * T_), 0, T_ * T_ * 2, 0x00020000);
    u32x2 mk = __builtin_amdgcn_raw_buffer_load_b64(mrs, (c & 7) * 16 + (g & 1) * 8, 0, 2);
    if (h == 0) {
      const u32x4 trs = make_srd((v ? vt : kt) + (size_t)bh * T_ * D_, T_ * D_ * 2);
      const int t0 = v ? vv0 : kv0, t1 = v ? vv0 : kv0 ^ 64;
      SET_M0(lds0 + TREE_OFF + v * 8192);
      DMA0(trs, t0, 0, 0); DMA0(trs, t1, 0, 1024);
    } else {
      *(LAS u32x4*)(lds + TREE_OFF + 8192 + 2048 + v * 1024 + lane * 16) = u32x4{0u, 0u, 0u, 0u};
      if (v == 0 && lane < 32) FULL[lane] = 0u;
    }
    LGKM0();
    unsigned cpf0;
    {
      const char* pcb = (const char*)(pc0 & ~(uintptr_t)255);
      const unsigned off = min(((blockIdx.x >> 3) * 4u + ci) * 128u, (unsigned)(CODEPF - 128));
      asm volatile("s_load_dword %0, %1, 0x0" : "=&s"(cpf0) : "s"(pcb + off));
    }
    __builtin_amdgcn_s_barrier();
    asm volatile("" : "+v"(mk) :: "memory");

    int ka[4], va[2];
#pragma unroll
    for (int s = 0; s < 4; ++s) ka[s] = c * 256 + (((4 * s + g) ^ c) << 4);
#pragma unroll
    for (int j = 0; j < 2; ++j) va[j] = (4 * g + (c >> 2)) * 256 + ((4 * ((2 * h + j) ^ (c >> 2)) + (c & 3)) << 4);
    LAS char* pst = lds + PS_OFF + v * 2048 + lane * 16;
    LAS char* mst = lds + AS_OFF + v * 128 + c * 8;
    volatile LAS float* lsum = (volatile LAS float*)(lds + XM_OFF + v * 128 + c * 4);

#define WAIT_FULL(need)                                                        \
  do {                                                                         \
    if (__builtin_amdgcn_readfirstlane(fpre) < (unsigned)(need)) {             \
      _Pragma("unroll 1") for (unsigned sp = 0; sp < SPIN_CAP; ++sp) {        \
        if (__builtin_amdgcn_readfirstlane(*FULL) >= (unsigned)(need)) break;  \
      }                                                                        \
    }                                                                          \
    MEMFENCE();                                                                \
  } while (0)
#define WAIT_FLAG(i)                                                           \
  do {                                                                         \
    _Pragma("unroll 1") for (unsigned sp = 0; sp < SPIN_CAP; ++sp)            \
      if (__builtin_amdgcn_readfirstlane(FLAG[i]) != 0u) break;                \
    MEMFENCE();                                                                \
  } while (0)
#define TRREAD(off) __builtin_bit_cast(u32x2, __builtin_amdgcn_ds_read_tr16_b64_v4i16((LAS s16x4*)(lds + (off))))
#define VREAD(vf, so)                                                                                                \
    _Pragma("unroll") for (int e = 0; e < 4; ++e) _Pragma("unroll") for (int u = 0; u < 2; ++u)                     \
        vf[e][u] = TRREAD((so) + va[e >> 1] + (e & 1) * 8 + u * 4096);
#define PVMMA(vf, P) _Pragma("unroll") for (int e = 0; e < 4; ++e) O[e] = mfma16(u32x4{vf[e][0][0], vf[e][0][1], vf[e][1][0], vf[e][1][1]}, P, O[e]);

    unsigned fpre = 0;
    float m, M;
    f32x4 L = {0.f, 0.f, 0.f, 0.f};
    const u32x4 ONES = {0x3f803f80u, 0x3f803f80u, 0x3f803f80u, 0x3f803f80u};
    f32x4 O[4];
    asm volatile("s_waitcnt lgkmcnt(0)" :: "s"(cpf0) : "memory");
    {
      asm volatile("s_waitcnt vmcnt(0)" ::: "memory");
      if (h == 0) FLAG[v] = 1u;
      WAIT_FLAG(0);
      WAIT_FLAG(1);
      f32x4 S = {0.f, 0.f, 0.f, 0.f};
#pragma unroll
      for (int s = 0; s < 4; ++s) S = mfma16(*(const LAS u32x4*)(lds + TREE_OFF + ka[s]), Q[s], S);
      const float NB = -1000000000.0f * LOG2E;
      const float mv[4] = {__uint_as_float(mk[0] << 16), __uint_as_float(mk[0] & 0xffff0000u), __uint_as_float(mk[1] << 16), __uint_as_float(mk[1] & 0xffff0000u)};
      float x[4], p[4];
#pragma unroll
      for (int i = 0; i < 4; ++i) x[i] = g < 2 ? fmaf(S[i], CS, (1.0f - mv[i]) * NB) : -INFINITY;
      M = m = __builtin_ceilf(xrow_max(fmaxf(fmaxf(x[0], x[1]), fmaxf(x[2], x[3]))));
#pragma unroll
      for (int i = 0; i < 4; ++i) p[i] = __builtin_amdgcn_exp2f(x[i] - m);
      const u32x4 P = {pack_bf16(p[0], p[1]), pack_bf16(p[2], p[3]), 0u, 0u};
      if (h == 0) L = mfma16(ONES, P, L);
#pragma unroll
      for (int e = 0; e < 4; ++e) {
        const u32x2 t = TRREAD(TREE_OFF + 8192 + va[e >> 1] + (e & 1) * 8);
        O[e] = mfma16(u32x4{t[0], t[1], 0u, 0u}, P, f32x4{0.f, 0.f, 0.f, 0.f});
      }
      LGKM0();
    }
    DONE[ci] = 1;

#define EXCHANGE() \
        if (ci == 2 && r >= JA - 1 && r <= JB + 6) { \
          const unsigned now = (unsigned)__builtin_amdgcn_s_memrealtime(); \
          if (r >= JA && now - tl > 160u) stall += now - tl - 54u;     \
          tl = now; \
          if (r == JB - 1) { stw = stall; stall = 0; }     \
          if (r == JB) { \
            ta = FULL[2] + stw; \
            tb = FULL[3]; \
            if (lane == 0) __hip_atomic_store(prog + blockIdx.x, ((unsigned long long)tb << 32) | ta, __ATOMIC_RELAXED, __HIP_MEMORY_SCOPE_AGENT); \
          } \
          if (r == JB + 4) { \
            const __amdgpu_buffer_rsrc_t prs = __builtin_amdgcn_make_buffer_rsrc((void*)prog, 0, NWG * 8, 0x00020000); \
            pe[0] = __builtin_amdgcn_raw_buffer_load_b128(prs, lane * 32, 0, 16); \
            pe[1] = __builtin_amdgcn_raw_buffer_load_b128(prs, lane * 32 + 16, 0, 16); \
          } \
          if (r == JB + 6) { \
            float n = 0.f, sa = 0.f, sb = 0.f; \
          _Pragma("unroll") \
            for (int j = 0; j < 4; ++j) { \
              const int da = (int)(pe[j >> 1][2 * (j & 1)] - ta), db = (int)(pe[j >> 1][2 * (j & 1) + 1] - tb); \
              const bool ok = db > -400 && db < 400 && db - da > -150 && db - da < 150; \
              n += ok ? 1.f : 0.f; sa += ok ? (float)da : 0.f; sb += ok ? (float)db : 0.f; \
            } \
            n = wave_sum(n); sa = wave_sum(sa); sb = wave_sum(sb); \
            if (n >= 128.f) { \
              const float rn = __builtin_amdgcn_rcpf(n); \
              const float per = ((float)(int)(tb - ta) + (sb - sa) * rn) * (16.0f * PMARG / (JB - JA)); \
              const unsigned sb0 = ((tb + stall) << 4) + (unsigned)(int)(sb * rn * 16.0f - per * (JB + 0.1f)); \
              if (lane == 0) *(volatile LAS u32x2*)SCHED = u32x2{sb0, (unsigned)per}; \
            } \
          } \
        }
    unsigned ta = 0, tb = 0, tl = 0, stall = 0, stw = 0;
    u32x4 pe[2] = {{0u, 0u, 0u, 0u}, {0u, 0u, 0u, 0u}};
    int ro = 0, r = 0;
    u32x2 vf[4][2], vg[4][2];
    u32x4 P0, P1;
    f32x2 mp;
#define NEXT_ROUND() { DONE[ci] = r + 2; ro = ro == RING - 16384 ? 0 : ro + 16384; ++r; }
#pragma unroll 1
    for (int half = 0; half < 2; ++half) {
#pragma unroll 1
      for (int k = 0; k < PH; ++k) {
        WAIT_FULL(16 * r + 16);
        u32x4 kf[2][4];
#pragma unroll
        for (int s = 0; s < 4; ++s)
#pragma unroll
          for (int u = 0; u < 2; ++u) kf[u][s] = *(const LAS u32x4*)(lds + ro + h * 8192 + ka[s] + u * 4096);
        SCHEDB();
        f32x4 S[2];
#pragma unroll
        for (int s = 0; s < 4; ++s)
#pragma unroll
          for (int u = 0; u < 2; ++u) S[u] = mfma16(kf[u][s], Q[s], s == 0 ? f32x4{0.f, 0.f, 0.f, 0.f} : S[u]);
        LGKM0();
        NEXT_ROUND()
        const float mr = max3(max3(S[0][0], S[0][1], S[0][2]), max3(S[0][3], S[1][0], S[1][1]), fmaxf(S[1][2], S[1][3]));
        const float mx = fmaxf(m, __builtin_ceilf(xrow_max(mr) * CS));
        const float alpha = __builtin_amdgcn_exp2f(m - mx);
        m = mx;
        if (g == 0) *(LAS float*)(mst + k * 256 + h * 4) = mx;
        float p[8];
#pragma unroll
        for (int i = 0; i < 8; ++i) p[i] = exp2c(fmaf(S[i >> 2][i & 3], CS, -mx));
        P0 = u32x4{pack_bf16(p[0], p[1]), pack_bf16(p[2], p[3]), pack_bf16(p[4], p[5]), pack_bf16(p[6], p[7])};
        L = mfma16(ONES, P0, L * alpha);
        *(LAS u32x4*)(pst + k * 4096 + h * 1024) = P0;
        fpre = *FULL;
        if (JB <= PH) { EXCHANGE() }
      }
      if (g == 0) lsum[16 * h] = L[0];
#define RESCALE()                                                                                                    \
        {                                                                                                            \
          const float Mn = max3(M, mp[0], mp[1]);                                                                    \
          if (__builtin_amdgcn_ballot_w64(M != Mn || mp[0] != Mn || mp[1] != Mn) != 0) {                             \
            const float a = __builtin_amdgcn_exp2f(M - Mn), s0 = __builtin_amdgcn_exp2f(mp[0] - Mn), s1 = __builtin_amdgcn_exp2f(mp[1] - Mn); \
            _Pragma("unroll") for (int e = 0; e < 4; ++e) {                                                         \
              O[e] *= a;                                                                                             \
              P0[e] = pack_bf16(__uint_as_float(P0[e] << 16) * s0, __uint_as_float(P0[e] & 0xffff0000u) * s0);       \
              P1[e] = pack_bf16(__uint_as_float(P1[e] << 16) * s1, __uint_as_float(P1[e] & 0xffff0000u) * s1);       \
            }                                                                                                        \
            M = Mn;                                                                                                  \
          }                                                                                                          \
          asm volatile("" : "+v"(O[0]), "+v"(O[1]), "+v"(O[2]), "+v"(O[3]), "+v"(P0), "+v"(P1));                     \
        }
      P0 = *(const LAS u32x4*)(pst);
      P1 = *(const LAS u32x4*)(pst + 1024);
      mp = *(const LAS f32x2*)(mst);
#pragma unroll 1
      for (int k = 1; k < PH; ++k) {
        RESCALE()
        WAIT_FULL(16 * r + 16);
        VREAD(vf, ro)
        VREAD(vg, ro + 8192)
        const u32x4 P0n = *(const LAS u32x4*)(pst + k * 4096), P1n = *(const LAS u32x4*)(pst + k * 4096 + 1024);
        mp = *(const LAS f32x2*)(mst + k * 256);
        fpre = *FULL;
        SCHEDB();
        PVMMA(vf, P0)
        PVMMA(vg, P1)
        SCHEDB();
        LGKM0();
        NEXT_ROUND()
        P0 = P0n;
        P1 = P1n;
        if (JB + 6 > PH) { EXCHANGE() }
      }
      const __amdgpu_buffer_rsrc_t ors = __builtin_amdgcn_make_buffer_rsrc((void*)(out + ((size_t)bh * M_ + 16 * v) * D_), 0, half ? 16 * D_ * 2 : 0, 0x00020000);
      RESCALE()
      float inv = __builtin_amdgcn_rcpf(fmaf(lsum[0], __builtin_amdgcn_exp2f(mp[0] - M), lsum[16] * __builtin_amdgcn_exp2f(mp[1] - M)));
      asm volatile("" : "+v"(inv));
      WAIT_FULL(16 * r + 8);
      VREAD(vf, ro)
      fpre = *FULL;
      SCHEDB();
      PVMMA(vf, P0)
      SCHEDB();
      WAIT_FULL(16 * r + 16);
      VREAD(vg, ro + 8192)
      SCHEDB();
      PVMMA(vg, P1)
      SCHEDB();
#pragma unroll
      for (int e2 = 0; e2 < 2; ++e2) {
        const f32x4 a = O[2 * e2] * inv, b = O[2 * e2 + 1] * inv;
        __builtin_amdgcn_raw_buffer_store_b128(u32x4{pack_bf16(a[0], a[1]), pack_bf16(a[2], a[3]), pack_bf16(b[0], b[1]), pack_bf16(b[2], b[3])},
                                               ors, c * 256 + g * 16 + h * 128 + e2 * 64, 0, 19);
      }
      LGKM0();
      NEXT_ROUND()
    }
  }
}
}

extern "C" void kernel_launch(void* const* d_in, const int* in_sizes, int n_in, void* d_out, int out_size, void* d_ws,
                              size_t ws_size, hipStream_t stream) {
  const u16* q = (const u16*)d_in[0];
  const u16* kc = (const u16*)d_in[1];
  const u16* vc = (const u16*)d_in[2];
  const u16* kt = (const u16*)d_in[3];
  const u16* vt = (const u16*)d_in[4];
  const u16* mask = (const u16*)d_in[5];
  (void)hipFuncSetAttribute((const void*)tree_attn_kernel, hipFuncAttributeMaxDynamicSharedMemorySize, LDS_BYTES);
  tree_attn_kernel<<<dim3(NWG), dim3(320), LDS_BYTES, stream>>>(q, kc, vc, kt, vt, mask, (u16*)d_out, (unsigned long long*)d_ws);
}
```

```cpp
#include <hip/hip_runtime.h>
#include <hip/hip_bf16.h>
#include <stdint.h>

#define WINL 20
#define WINB 32

namespace {
constexpr int B_ = 32, HQ = 32, HKV = 8, T_ = 8, C_ = 2048, D_ = 128;
constexpr int G_ = HQ / HKV;
constexpr int M_ = G_ * T_;
constexpr int NWG = B_ * HKV;
constexpr int PH = 16;
constexpr int NROUND = 4 * PH;
constexpr int NPIECE = NROUND * 16;
constexpr int JA = 6, JB = 14;
constexpr float PMARG = 0.994f;
constexpr unsigned DELTA = 400;
static_assert(M_ == 32 && D_ == 128 && T_ == 8 && NWG == 256 && C_ == 2 * PH * 64 && WINL >= 17 && WINL <= 24 && WINB <= 32 && JB % 2 == 0, "layout");

typedef __attribute__((ext_vector_type(8))) __bf16 bf16x8;
typedef __attribute__((ext_vector_type(4))) float f32x4;
typedef __attribute__((ext_vector_type(2))) float f32x2;
typedef __attribute__((ext_vector_type(4))) unsigned u32x4;
typedef __attribute__((ext_vector_type(2))) unsigned u32x2;
typedef __attribute__((ext_vector_type(4))) short s16x4;
typedef unsigned short u16;
#define LAS __attribute__((address_space(3)))

constexpr float LOG2E = 1.4426950408889634f;
constexpr float SCALE = 0.08838834764831845f;
constexpr float CS = SCALE * LOG2E;

constexpr int RING = 5 * 16384, TREE_OFF = 4 * 16384, PS_OFF = RING, AS_OFF = PS_OFF + PH * 4096, XM_OFF = AS_OFF + PH * 256, CTL_OFF = XM_OFF + 512;
constexpr int LDS_BYTES = CTL_OFF + 128;
constexpr unsigned SPIN_CAP = 1u << 18;

__device__ __forceinline__ f32x4 mfma16(u32x4 a, u32x4 b, f32x4 c) {
  return __builtin_amdgcn_mfma_f32_16x16x32_bf16(__builtin_bit_cast(bf16x8, a), __builtin_bit_cast(bf16x8, b), c, 0, 0, 0);
}
__device__ __forceinline__ unsigned pack_bf16(float lo, float hi) {
  typedef __attribute__((ext_vector_type(2))) __bf16 bf16x2;
  f32x2 v = {lo, hi};
  bf16x2 r = __builtin_convertvector(v, bf16x2);
  return __builtin_bit_cast(unsigned, r);
}
__device__ __forceinline__ float max3(float a, float b, float c) { return fmaxf(fmaxf(a, b), c); }
__device__ __forceinline__ float exp2c(float x) { return __builtin_amdgcn_fmed3f(__builtin_amdgcn_exp2f(x), 0.f, 1.f); }
__device__ __forceinline__ float xrow_max(float x) {
  auto s = __builtin_amdgcn_permlane16_swap(__float_as_uint(x), __float_as_uint(x), false, false);
  x = fmaxf(__uint_as_float(s[0]), __uint_as_float(s[1]));
  auto t = __builtin_amdgcn_permlane32_swap(__float_as_uint(x), __float_as_uint(x), false, false);
  return fmaxf(__uint_as_float(t[0]), __uint_as_float(t[1]));
}
__device__ __forceinline__ float xrow_sum(float x) {
  auto s = __builtin_amdgcn_permlane16_swap(__float_as_uint(x), __float_as_uint(x), false, false);
  x = __uint_as_float(s[0]) + __uint_as_float(s[1]);
  auto t = __builtin_amdgcn_permlane32_swap(__float_as_uint(x), __float_as_uint(x), false, false);
  return __uint_as_float(t[0]) + __uint_as_float(t[1]);
}
__device__ __forceinline__ float wave_sum(float x) {
  x += __builtin_bit_cast(float, __builtin_amdgcn_mov_dpp(__builtin_bit_cast(int, x), 0xB1, 0xf, 0xf, true));
  x += __builtin_bit_cast(float, __builtin_amdgcn_mov_dpp(__builtin_bit_cast(int, x), 0x4E, 0xf, 0xf, true));
  x += __builtin_bit_cast(float, __builtin_amdgcn_mov_dpp(__builtin_bit_cast(int, x), 0x141, 0xf, 0xf, true));
  x += __builtin_bit_cast(float, __builtin_amdgcn_mov_dpp(__builtin_bit_cast(int, x), 0x140, 0xf, 0xf, true));
  return xrow_sum(x);
}
__device__ __forceinline__ u32x4 make_srd(const void* p, unsigned bytes) {
  const uint64_t a = (uint64_t)p;
  return u32x4{(unsigned)a, (unsigned)(a >> 32) & 0xffffu, bytes, 0x00020000u};
}
#define STR_(x) #x
#define STR(x) STR_(x)
#define SET_M0(x) asm volatile("s_mov_b32 m0, %0\n\ts_nop 0" :: "s"(x) : "memory")
#define DMAW(srd, voff, soff, IMM, W)                                                                            \
  asm volatile("buffer_load_dwordx4 %0, %1, %2 offen offset:" #IMM " nt lds\n\ts_waitcnt vmcnt(" STR(W) ")"      \
               :: "v"(voff), "s"(srd), "s"(soff) : "memory")
#define DMA(srd, voff, soff, IMM) DMAW(srd, voff, soff, IMM, WINL)
#define DMAB(srd, voff, soff, IMM) DMAW(srd, voff, soff, IMM, WINB)
#define DMA0(srd, voff, soff, IMM) DMAW(srd, voff, soff, IMM, 63)
#define MEMFENCE() asm volatile("" ::: "memory")
#define LGKM0() do { MEMFENCE(); __builtin_amdgcn_s_waitcnt(0xc07f); MEMFENCE(); } while (0)
#define SCHEDB() __builtin_amdgcn_sched_barrier(0)

__global__ __launch_bounds__(320, 2) void tree_attn_kernel(const u16* q, const u16* kc, const u16* vc, const u16* kt, const u16* vt,
                                                            const u16* mask, u16* out, unsigned long long* prog) {
  extern __shared__ __attribute__((aligned(16))) char lds_generic[];
  LAS char* lds = (LAS char*)lds_generic;
  const int bh = (blockIdx.x & 7) * 32 + (blockIdx.x >> 3);
  const int lane = threadIdx.x & 63;
  const int wave = __builtin_amdgcn_readfirstlane(threadIdx.x >> 6);
  const int g = lane >> 4, c = lane & 15;
  volatile LAS unsigned* FULL = (volatile LAS unsigned*)(lds + CTL_OFF);
  volatile LAS unsigned* DONE = (volatile LAS unsigned*)(lds + CTL_OFF + 16);
  volatile LAS unsigned* SCHED = (volatile LAS unsigned*)(lds + CTL_OFF + 32);
  volatile LAS unsigned* FLAG = (volatile LAS unsigned*)(lds + CTL_OFF + 48);

  const unsigned lds0 = (unsigned)(uintptr_t)lds;
  const size_t base = (size_t)bh * C_ * D_;
  const int kv0 = g * 256 + ((c ^ g) << 4), vv0 = g * 256 + ((c ^ (g << 2)) << 4);
  u32x4 srd = make_srd(kc + base, C_ * D_ * 2);
  int o0 = kv0, o1 = kv0 ^ 64, o2 = kv0 ^ 128, o3 = kv0 ^ 192;
#define BATCH(D, go, ro, P, i)                                                                                       \
    SET_M0(lds0 + (ro));                                                                                             \
    D(srd, o0, go, 0); P(i + 1) D(srd, o1, go, 1024); P(i + 2) D(srd, o2, go, 2048); P(i + 3) D(srd, o3, go, 3072); P(i + 4)
#define ROUND(D, go, ro, P) BATCH(D, go, ro, P, 0) BATCH(D, go + 4096, ro + 4096, P, 4) BATCH(D, go + 8192, ro + 8192, P, 8) BATCH(D, go + 12288, ro + 12288, P, 12)
#define NOPUB(i)
#define PUB(i) if ((i) == WINL - 16) *FULL = 16 * r - 16;
#define PUBB(i) if ((i) == WINB - 16) *FULL = 16 * r - 16;
  if (wave == 0) {
    ROUND(DMA0, 0, 0, NOPUB)
  }
  MEMFENCE();
  if (wave == 0) {
    __builtin_amdgcn_s_setprio(3);
    __builtin_amdgcn_s_barrier();
    MEMFENCE();
    unsigned dmin = 0;
    unsigned sbase = 0, sper = 0;
    int ro = 16384, early = 0, plag = 1 << 24;
    unsigned adj = 0;
#pragma unroll 1
    for (int r = 1; r < NROUND; ++r) {
      if ((r & (PH - 1)) == 0) {
        const bool isv = (r / PH) & 1;
        srd = make_srd((isv ? vc : kc) + base, C_ * D_ * 2);
        o0 = isv ? vv0 : kv0; o1 = isv ? vv0 : kv0 ^ 64; o2 = isv ? vv0 : kv0 ^ 128; o3 = isv ? vv0 : kv0 ^ 192;
      }
      if (__builtin_expect((int)dmin < r - 3, 0)) {
#pragma unroll 1
        for (unsigned sp = 0; sp < SPIN_CAP; ++sp) {
          dmin = __builtin_amdgcn_readfirstlane(min(min(DONE[0], DONE[1]), min(DONE[2], DONE[3])));
          if ((int)dmin >= r - 3) break;
        }
        MEMFENCE();
      }
      const int go = ((r / (2 * PH)) * PH + (r & (PH - 1))) * 16384;
      int lag = 0;
      if (sper && early < 5) {
        const unsigned due = sbase + r * sper + (r / PH) * DELTA + adj;
        lag = (int)(((unsigned)__builtin_amdgcn_s_memrealtime() << 4) - due);
        early = lag < -(int)(sper >> 2) ? early + 1 : 0;
#pragma unroll 1
        for (int sp = 0; sp < 64 && lag < 0; ++sp) lag = (int)(((unsigned)__builtin_amdgcn_s_memrealtime() << 4) - due);
        if (((r + 3) & (PH - 1)) > 6 && lag - plag > 1600) { adj += lag - plag; lag = plag; }
        plag = lag;
      }
      u32x4 cw;
      u32x2 sw;
      if (lag > 80) {
        BATCH(DMAB, go, ro, PUBB, 0) BATCH(DMAB, go + 4096, ro + 4096, PUBB, 4) BATCH(DMAB, go + 8192, ro + 8192, PUBB, 8)
        cw = *(volatile LAS u32x4*)DONE;
        sw = *(volatile LAS u32x2*)SCHED;
        BATCH(DMAB, go + 12288, ro + 12288, PUBB, 12)
      } else {
        BATCH(DMA, go, ro, PUB, 0)
        if (r == JA || r == JB) FULL[r == JA ? 2 : 3] = (unsigned)__builtin_amdgcn_s_memrealtime();
        BATCH(DMA, go + 4096, ro + 4096, PUB, 4) BATCH(DMA, go + 8192, ro + 8192, PUB, 8)
        cw = *(volatile LAS u32x4*)DONE;
        sw = *(volatile LAS u32x2*)SCHED;
        BATCH(DMA, go + 12288, ro + 12288, PUB, 12)
      }
      ro = ro == RING - 16384 ? 0 : ro + 16384;
      dmin = __builtin_amdgcn_readfirstlane(min(min(cw[0], cw[1]), min(cw[2], cw[3])));
      sbase = __builtin_amdgcn_readfirstlane(sw[0]);
      sper = __builtin_amdgcn_readfirstlane(sw[1]);
    }
#define DRAIN(n) asm volatile("s_waitcnt vmcnt(" #n ")" ::: "memory"); *FULL = NPIECE - n;
    DRAIN(16) DRAIN(8) DRAIN(0)
  } else {
    const int ci = wave - 1, v = ci & 1, h = ci >> 1;
    u32x4 Q[4];
    {
      const __amdgpu_buffer_rsrc_t qrs = __builtin_amdgcn_make_buffer_rsrc((void*)(q + ((size_t)bh * M_ + 16 * v) * D_), 0, 16 * D_ * 2, 0x00020000);
#pragma unroll
      for (int s = 0; s < 4; ++s) Q[s] = __builtin_amdgcn_raw_buffer_load_b128(qrs, c * 256 + g * 16 + s * 64, 0, 2);
    }
    const __amdgpu_buffer_rsrc_t mrs = __builtin_amdgcn_make_buffer_rsrc((void*)(mask + (size_t)(bh / HKV) * T_ * T_), 0, T_ * T_ * 2, 0x00020000);
    u32x2 mk = __builtin_amdgcn_raw_buffer_load_b64(mrs, (c & 7) * 16 + (g & 1) * 8, 0, 2);
    if (h == 0) {
      const u32x4 trs = make_srd((v ? vt : kt) + (size_t)bh * T_ * D_, T_ * D_ * 2);
      const int t0 = v ? vv0 : kv0, t1 = v ? vv0 : kv0 ^ 64;
      SET_M0(lds0 + TREE_OFF + v * 8192);
      DMA0(trs, t0, 0, 0); DMA0(trs, t1, 0, 1024);
    } else {
      *(LAS u32x4*)(lds + TREE_OFF + 8192 + 2048 + v * 1024 + lane * 16) = u32x4{0u, 0u, 0u, 0u};
      if (v == 0 && lane < 32) FULL[lane] = 0u;
    }
    LGKM0();
    unsigned tw0;
    {
      const void* tp = ci == 0 ? (const void*)(vc + base) : (ci == 1 ? (const void*)(out + (size_t)bh * M_ * D_) : (const void*)(prog + NWG));
      asm volatile("s_load_dword %0, %1, 0x0" : "=&s"(tw0) : "s"(tp));
    }
    __builtin_amdgcn_s_barrier();
    asm volatile("" : "+v"(mk) :: "memory");
    asm volatile("s_waitcnt lgkmcnt(0)" :: "s"(tw0) : "memory");

    int ka[4], va[2];
#pragma unroll
    for (int s = 0; s < 4; ++s) ka[s] = c * 256 + (((4 * s + g) ^ c) << 4);
#pragma unroll
    for (int j = 0; j < 2; ++j) va[j] = (4 * g + (c >> 2)) * 256 + ((4 * ((2 * h + j) ^ (c >> 2)) + (c & 3)) << 4);
    LAS char* pst = lds + PS_OFF + v * 2048 + lane * 16;
    LAS char* mst = lds + AS_OFF + v * 128 + c * 8;
    volatile LAS float* lsum = (volatile LAS float*)(lds + XM_OFF + v * 128 + c * 4);

#define WAIT_FULL(need)                                                        \
  do {                                                                         \
    if (__builtin_amdgcn_readfirstlane(fpre) < (unsigned)(need)) {             \
      _Pragma("unroll 1") for (unsigned sp = 0; sp < SPIN_CAP; ++sp) {        \
        if (__builtin_amdgcn_readfirstlane(*FULL) >= (unsigned)(need)) break;  \
      }                                                                        \
    }                                                                          \
    MEMFENCE();                                                                \
  } while (0)
#define WAIT_FLAG(i)                                                           \
  do {                                                                         \
    _Pragma("unroll 1") for (unsigned sp = 0; sp < SPIN_CAP; ++sp)            \
      if (__builtin_amdgcn_readfirstlane(FLAG[i]) != 0u) break;                \
    MEMFENCE();                                                                \
  } while (0)
#define TRREAD(off) __builtin_bit_cast(u32x2, __builtin_amdgcn_ds_read_tr16_b64_v4i16((LAS s16x4*)(lds + (off))))
#define VREAD(vf, so)                                                                                                \
    _Pragma("unroll") for (int e = 0; e < 4; ++e) _Pragma("unroll") for (int u = 0; u < 2; ++u)                     \
        vf[e][u] = TRREAD((so) + va[e >> 1] + (e & 1) * 8 + u * 4096);
#define PVMMA(vf, P) _Pragma("unroll") for (int e = 0; e < 4; ++e) O[e] = mfma16(u32x4{vf[e][0][0], vf[e][0][1], vf[e][1][0], vf[e][1][1]}, P, O[e]);

    unsigned fpre = 0;
    float m, M;
    f32x4 L = {0.f, 0.f, 0.f, 0.f};
    const u32x4 ONES = {0x3f803f80u, 0x3f803f80u, 0x3f803f80u, 0x3f803f80u};
    f32x4 O[4];
    {
      asm volatile("s_waitcnt vmcnt(0)" ::: "memory");
      if (h == 0) FLAG[v] = 1u;
      WAIT_FLAG(0);
      WAIT_FLAG(1);
      f32x4 S = {0.f, 0.f, 0.f, 0.f};
#pragma unroll
      for (int s = 0; s < 4; ++s) S = mfma16(*(const LAS u32x4*)(lds + TREE_OFF + ka[s]), Q[s], S);
      const float NB = -1000000000.0f * LOG2E;
      const float mv[4] = {__uint_as_float(mk[0] << 16), __uint_as_float(mk[0] & 0xffff0000u), __uint_as_float(mk[1] << 16), __uint_as_float(mk[1] & 0xffff0000u)};
      float x[4], p[4];
#pragma unroll
      for (int i = 0; i < 4; ++i) x[i] = g < 2 ? fmaf(S[i], CS, (1.0f - mv[i]) * NB) : -INFINITY;
      M = m = __builtin_ceilf(xrow_max(fmaxf(fmaxf(x[0], x[1]), fmaxf(x[2], x[3]))));
#pragma unroll
      for (int i = 0; i < 4; ++i) p[i] = __builtin_amdgcn_exp2f(x[i] - m);
      const u32x4 P = {pack_bf16(p[0], p[1]), pack_bf16(p[2], p[3]), 0u, 0u};
      if (h == 0) L = mfma16(ONES, P, L);
#pragma unroll
      for (int e = 0; e < 4; ++e) {
        const u32x2 t = TRREAD(TREE_OFF + 8192 + va[e >> 1] + (e & 1) * 8);
        O[e] = mfma16(u32x4{t[0], t[1], 0u, 0u}, P, f32x4{0.f, 0.f, 0.f, 0.f});
      }
      LGKM0();
    }
    DONE[ci] = 1;

#define EXCHANGE() \
        if (ci == 2 && r >= JA - 1 && r <= JB + 6) { \
          const unsigned now = (unsigned)__builtin_amdgcn_s_memrealtime(); \
          if (r >= JA && now - tl > 160u) stall += now - tl - 54u;     \
          tl = now; \
          if (r == JB - 1) stw = stall;     \
          if (r == JB) { \
            ta = FULL[2] + stw; \
            tb = FULL[3]; \
            if (lane == 0) __hip_atomic_store(prog + blockIdx.x, ((unsigned long long)tb << 32) | ta, __ATOMIC_RELAXED, __HIP_MEMORY_SCOPE_AGENT); \
          } \
          if (r == JB + 4) { \
            const __amdgpu_buffer_rsrc_t prs = __builtin_amdgcn_make_buffer_rsrc((void*)prog, 0, NWG * 8, 0x00020000); \
            pe[0] = __builtin_amdgcn_raw_buffer_load_b128(prs, lane * 32, 0, 16); \
            pe[1] = __builtin_amdgcn_raw_buffer_load_b128(prs, lane * 32 + 16, 0, 16); \
          } \
          if (r == JB + 6) { \
            float n = 0.f, sa = 0.f, sb = 0.f; \
          _Pragma("unroll") \
            for (int j = 0; j < 4; ++j) { \
              const int da = (int)(pe[j >> 1][2 * (j & 1)] - ta), db = (int)(pe[j >> 1][2 * (j & 1) + 1] - tb); \
              const bool ok = db > -400 && db < 400 && db - da > -150 && db - da < 150; \
              n += ok ? 1.f : 0.f; sa += ok ? (float)da : 0.f; sb += ok ? (float)db : 0.f; \
            } \
            n = wave_sum(n); sa = wave_sum(sa); sb = wave_sum(sb); \
            if (n >= 128.f) { \
              const float rn = __builtin_amdgcn_rcpf(n); \
              const float per = ((float)(int)(tb - ta) + (sb - sa) * rn) * (16.0f * PMARG / (JB - JA)); \
              const unsigned sb0 = (tb << 4) + (unsigned)(int)(sb * rn * 16.0f - per * (JB + 0.1f)); \
              if (lane == 0) *(volatile LAS u32x2*)SCHED = u32x2{sb0, (unsigned)per}; \
            } \
          } \
        }
    unsigned ta = 0, tb = 0, tl = 0, stall = 0, stw = 0;
    u32x4 pe[2] = {{0u, 0u, 0u, 0u}, {0u, 0u, 0u, 0u}};
    int ro = 0, r = 0;
    u32x2 vf[4][2], vg[4][2];
    u32x4 P0, P1;
    f32x2 mp;
#define NEXT_ROUND() { DONE[ci] = r + 2; ro = ro == RING - 16384 ? 0 : ro + 16384; ++r; }
#pragma unroll 1
    for (int half = 0; half < 2; ++half) {
#pragma unroll 1
      for (int k = 0; k < PH; ++k) {
        WAIT_FULL(16 * r + 16);
        u32x4 kf[2][4];
#pragma unroll
        for (int s = 0; s < 4; ++s)
#pragma unroll
          for (int u = 0; u < 2; ++u) kf[u][s] = *(const LAS u32x4*)(lds + ro + h * 8192 + ka[s] + u * 4096);
        SCHEDB();
        f32x4 S[2];
#pragma unroll
        for (int s = 0; s < 4; ++s)
#pragma unroll
          for (int u = 0; u < 2; ++u) S[u] = mfma16(kf[u][s], Q[s], s == 0 ? f32x4{0.f, 0.f, 0.f, 0.f} : S[u]);
        LGKM0();
        NEXT_ROUND()
        const float mr = max3(max3(S[0][0], S[0][1], S[0][2]), max3(S[0][3], S[1][0], S[1][1]), fmaxf(S[1][2], S[1][3]));
        const float mx = fmaxf(m, __builtin_ceilf(xrow_max(mr) * CS));
        const float alpha = __builtin_amdgcn_exp2f(m - mx);
        m = mx;
        if (g == 0) *(LAS float*)(mst + k * 256 + h * 4) = mx;
        float p[8];
#pragma unroll
        for (int i = 0; i < 8; ++i) p[i] = exp2c(fmaf(S[i >> 2][i & 3], CS, -mx));
        P0 = u32x4{pack_bf16(p[0], p[1]), pack_bf16(p[2], p[3]), pack_bf16(p[4], p[5]), pack_bf16(p[6], p[7])};
        L = mfma16(ONES, P0, L * alpha);
        *(LAS u32x4*)(pst + k * 4096 + h * 1024) = P0;
        fpre = *FULL;
        if (JB <= PH) { EXCHANGE() }
      }
      if (g == 0) lsum[16 * h] = L[0];
#define RESCALE()                                                                                                    \
        {                                                                                                            \
          const float Mn = max3(M, mp[0], mp[1]);                                                                    \
          if (__builtin_amdgcn_ballot_w64(M != Mn || mp[0] != Mn || mp[1] != Mn) != 0) {                             \
            const float a = __builtin_amdgcn_exp2f(M - Mn), s0 = __builtin_amdgcn_exp2f(mp[0] - Mn), s1 = __builtin_amdgcn_exp2f(mp[1] - Mn); \
            _Pragma("unroll") for (int e = 0; e < 4; ++e) {                                                         \
              O[e] *= a;                                                                                             \
              P0[e] = pack_bf16(__uint_as_float(P0[e] << 16) * s0, __uint_as_float(P0[e] & 0xffff0000u) * s0);       \
              P1[e] = pack_bf16(__uint_as_float(P1[e] << 16) * s1, __uint_as_float(P1[e] & 0xffff0000u) * s1);       \
            }                                                                                                        \
            M = Mn;                                                                                                  \
          }                                                                                                          \
          asm volatile("" : "+v"(O[0]), "+v"(O[1]), "+v"(O[2]), "+v"(O[3]), "+v"(P0), "+v"(P1));                     \
        }
      P0 = *(const LAS u32x4*)(pst);
      P1 = *(const LAS u32x4*)(pst + 1024);
      mp = *(const LAS f32x2*)(mst);
#pragma unroll 1
      for (int k = 1; k < PH; ++k) {
        RESCALE()
        WAIT_FULL(16 * r + 16);
        VREAD(vf, ro)
        VREAD(vg, ro + 8192)
        const u32x4 P0n = *(const LAS u32x4*)(pst + k * 4096), P1n = *(const LAS u32x4*)(pst + k * 4096 + 1024);
        mp = *(const LAS f32x2*)(mst + k * 256);
        fpre = *FULL;
        SCHEDB();
        PVMMA(vf, P0)
        PVMMA(vg, P1)
        SCHEDB();
        LGKM0();
        NEXT_ROUND()
        P0 = P0n;
        P1 = P1n;
        if (JB + 6 > PH) { EXCHANGE() }
      }
      const __amdgpu_buffer_rsrc_t ors = __builtin_amdgcn_make_buffer_rsrc((void*)(out + ((size_t)bh * M_ + 16 * v) * D_), 0, half ? 16 * D_ * 2 : 0, 0x00020000);
      RESCALE()
      float inv = __builtin_amdgcn_rcpf(fmaf(lsum[0], __builtin_amdgcn_exp2f(mp[0] - M), lsum[16] * __builtin_amdgcn_exp2f(mp[1] - M)));
      asm volatile("" : "+v"(inv));
      WAIT_FULL(16 * r + 8);
      VREAD(vf, ro)
      fpre = *FULL;
      SCHEDB();
      PVMMA(vf, P0)
      SCHEDB();
      WAIT_FULL(16 * r + 16);
      VREAD(vg, ro + 8192)
      SCHEDB();
      PVMMA(vg, P1)
      SCHEDB();
#pragma unroll
      for (int e2 = 0; e2 < 2; ++e2) {
        const f32x4 a = O[2 * e2] * inv, b = O[2 * e2 + 1] * inv;
        __builtin_amdgcn_raw_buffer_store_b128(u32x4{pack_bf16(a[0], a[1]), pack_bf16(a[2], a[3]), pack_bf16(b[0], b[1]), pack_bf16(b[2], b[3])},
                                               ors, c * 256 + g * 16 + h * 128 + e2 * 64, 0, 19);
      }
      LGKM0();
      NEXT_ROUND()
    }
  }
}
}

extern "C" void kernel_launch(void* const* d_in, const int* in_sizes, int n_in, void* d_out, int out_size, void* d_ws,
                              size_t ws_size, hipStream_t stream) {
  const u16* q = (const u16*)d_in[0];
  const u16* kc = (const u16*)d_in[1];
  const u16* vc = (const u16*)d_in[2];
  const u16* kt = (const u16*)d_in[3];
  const u16* vt = (const u16*)d_in[4];
  const u16* mask = (const u16*)d_in[5];
  (void)hipFuncSetAttribute((const void*)tree_attn_kernel, hipFuncAttributeMaxDynamicSharedMemorySize, LDS_BYTES);
  tree_attn_kernel<<<dim3(NWG), dim3(320), LDS_BYTES, stream>>>(q, kc, vc, kt, vt, mask, (u16*)d_out, (unsigned long long*)d_ws);
}
```
